```python
import torch

def _softmax(x, axis=-1):
    return torch.softmax(x, dim=axis)
def _gelu(x):
    return torch.nn.functional.gelu(x, approximate="tanh")
def _silu(x):
    return torch.nn.functional.silu(x)
def _sigmoid(x):
    return torch.sigmoid(x)
def _layernorm(x, axis=-1, eps=1e-5):
    m = x.mean(dim=axis, keepdim=True)
    v = x.var(dim=axis, keepdim=True, unbiased=False)
    return (x - m) / torch.sqrt(v + eps)
def _scatter_add(tgt, idx, src, axis=0):
    return tgt.clone().index_add_(axis, idx, src)
def _segment_sum(src, seg, n):
    out = torch.zeros((n,) + src.shape[1:], dtype=src.dtype)
    return out.index_add_(0, seg, src)
def _pad(x, pw):
    flat = tuple(v for pair in reversed(pw) for v in pair)
    return torch.nn.functional.pad(x, flat)

kernel_name = 'interleave_layers__gqa_block_cached__glu_gated_geglu__decode'

def setup_inputs():

    _w = (lambda a: (lambda t: t.to(torch.bfloat16) if t.is_floating_point() else t)(torch.as_tensor(a)))
    def _A_setup():
        B, Sq, D = 1, 1, 576
        Hq, Hkv, Sk, Dh = 32, 16, 65536, 128
        assert Hq % Hkv == 0, (Hq, Hkv)
        return {
            "x": _w((lambda *s: torch.randn(*s, dtype=torch.bfloat16) * 0.05)(B, Sq, D)),
            "k_cache": _w((lambda *s: torch.randn(*s, dtype=torch.bfloat16) * 0.8)(B, Hkv, Sk, Dh)),
            "v_cache": _w((lambda *s: torch.randn(*s, dtype=torch.bfloat16) * 1.6)(B, Hkv, Sk, Dh)),
            "wq": _w((lambda *s: torch.randn(*s, dtype=torch.bfloat16) * 0.8)(D, Hq * Dh) * (D ** -0.5)),
            "wo": _w((lambda *s: torch.randn(*s, dtype=torch.bfloat16) * 1.6)(Hq * Dh, D) * ((Hq * Dh) ** -0.5)),
        }
    _A_in = _A_setup()

    def _B_setup():
        B, S, Dm, Df = 1, 1, 576, 4608
        return {
            "x": _w((lambda *s: torch.randn(*s, dtype=torch.bfloat16) * 0.05)(B, S, Dm)),
            "w_gate": _w((lambda *s: torch.randn(*s, dtype=torch.bfloat16) * 0.4)(Dm, Df) * (Dm ** -0.5)),
            "w_up": _w((lambda *s: torch.randn(*s, dtype=torch.bfloat16) * 0.4)(Dm, Df) * (Dm ** -0.5)),
            "w_down": _w((lambda *s: torch.randn(*s, dtype=torch.bfloat16) * 0.4)(Df, Dm) * (Df ** -0.5)),
        }
    _B_in = _B_setup()

    return {
        "A_x": _A_in["x"],
        "A_k_cache": _A_in["k_cache"],
        "A_v_cache": _A_in["v_cache"],
        "A_wq": _A_in["wq"],
        "A_wo": _A_in["wo"],
        "B_x": _B_in["x"],
        "B_w_gate": _B_in["w_gate"],
        "B_w_up": _B_in["w_up"],
        "B_w_down": _B_in["w_down"],
    }

def reference(A_x, A_k_cache, A_v_cache, A_wq, A_wo, B_x, B_w_gate, B_w_up, B_w_down):
    def _A_ref(x, k_cache, v_cache, wq, wo):
        B, Sq, D = x.shape
        Hq, Dh = 32, 128
        Hkv = k_cache.shape[1]
        rep = Hq // Hkv
        q = (lambda x, *d: x.permute(*d))((x @ wq).reshape(B, Sq, Hq, Dh), 0, 2, 1, 3)
        k = (lambda x, n, axis: torch.repeat_interleave(x, n, dim=axis))(k_cache, rep, axis=1)
        v = (lambda x, n, axis: torch.repeat_interleave(x, n, dim=axis))(v_cache, rep, axis=1)
        scores = torch.einsum("bhqd,bhkd->bhqk", q, k) * (Dh ** -0.5)
        p = _softmax(scores, axis=-1)
        o = torch.einsum("bhqk,bhkd->bhqd", p, v)
        return (lambda x, *d: x.permute(*d))(o, 0, 2, 1, 3).reshape(B, Sq, Hq * Dh) @ wo

    def _B_ref(x, w_gate, w_up, w_down):
        g = torch.einsum("bsd,df->bsf", x, w_gate)
        u = torch.einsum("bsd,df->bsf", x, w_up)
        h = _gelu(g) * u
        return torch.einsum("bsf,fd->bsd", h, w_down)

    x = A_x
    x = x + _A_ref(_layernorm(x, axis=-1), A_k_cache, A_v_cache, A_wq, A_wo)
    x = x + _B_ref(_layernorm(x, axis=-1), B_w_gate, B_w_up, B_w_down)
    x = x + _A_ref(_layernorm(x, axis=-1), A_k_cache, A_v_cache, A_wq, A_wo)
    x = x + _B_ref(_layernorm(x, axis=-1), B_w_gate, B_w_up, B_w_down)
    return x
```

```cpp
#include <hip/hip_runtime.h>
#include <hip/hip_bf16.h>
#include <type_traits>

#define DIAG 0

namespace {

typedef unsigned v4u __attribute__((ext_vector_type(4)));
typedef unsigned v2u __attribute__((ext_vector_type(2)));
typedef int v4i __attribute__((ext_vector_type(4)));
typedef float f4 __attribute__((ext_vector_type(4)));
typedef float f2 __attribute__((ext_vector_type(2)));
typedef __bf16 bf2 __attribute__((ext_vector_type(2)));
#define GAS __attribute__((address_space(1)))
#define FI __device__ __forceinline__

constexpr int DM = 576, DFF = 4608, NQ = 4096;
constexpr int NWG = 256;
constexpr int NMLP = 144;
constexpr int NTILE = 64;
constexpr int NR = 9;
constexpr int NL = 9;
#define LOOKAHEAD 2
constexpr int LA = LOOKAHEAD;
constexpr int RS = (LA + 1) / 2;
constexpr int NSTREAM = NTILE - NR - NL;
constexpr int B1 = 3, B2 = 7;
constexpr int T_RING = NR + NL - RS;
constexpr float QSCALE = 0.08838834764831845f * 1.4426950408889634f;
constexpr unsigned SPIN_CAP = 200000u;

constexpr unsigned WS_EPOCH = 0;
constexpr unsigned WS_QG = 4096;
constexpr unsigned WS_XG = 32768;
constexpr unsigned WS_PA = 65536, PA_STRIDE = 264 * 8;
constexpr unsigned WS_PB = 1u << 20, PB_STRIDE = DM * 8;
constexpr unsigned WS_BYTES = WS_PB + NWG * PB_STRIDE;
constexpr unsigned WS_DIAG = 4u << 20;

constexpr int L_KS = 0;
constexpr int L_SC = 4 * NL * 4096;
constexpr int L_REDB = L_SC;
constexpr int L_REDA = L_SC + 10240;
constexpr int L_XB = L_REDA + 2048;
constexpr int L_QB = L_XB + 1280;
constexpr int L_TAG = L_QB + 512;
constexpr int L_BYTES = L_TAG + 64;
static_assert(L_BYTES <= 163840, "LDS");

template <int B, int E, class F> FI void sfor(F&& f) {
  if constexpr (B < E) { f(std::integral_constant<int, B>{}); sfor<B + 1, E>(f); }
}

FI v4i mk_rsrc(const void* p, unsigned bytes) {
  unsigned long long a = (unsigned long long)p;
  v4i r = {(int)(unsigned)a, (int)((unsigned)(a >> 32) & 0xffffu), (int)bytes, 0x00020000};
  return r;
}
FI float bf_lo(unsigned u) { return __uint_as_float(u << 16); }
FI float bf_hi(unsigned u) { return __uint_as_float(u & 0xffff0000u); }
FI unsigned pk_bf(float a, float b) { f2 v = {a, b}; return __builtin_bit_cast(unsigned, __builtin_convertvector(v, bf2)); }
FI float rbf(float a) { return bf_lo(pk_bf(a, a)); }
FI float ex2(float x) { return __builtin_amdgcn_exp2f(x); }

template <int CTRL> FI float dpp(float x) {
  return __builtin_bit_cast(float, __builtin_amdgcn_mov_dpp(__builtin_bit_cast(int, x), CTRL, 0xf, 0xf, true));
}
FI float row_sum(float x) {
  x += dpp<0xB1>(x); x += dpp<0x4E>(x); x += dpp<0x141>(x); x += dpp<0x128>(x); return x;
}
FI float row_max(float x) {
  x = fmaxf(x, dpp<0xB1>(x)); x = fmaxf(x, dpp<0x4E>(x)); x = fmaxf(x, dpp<0x141>(x)); x = fmaxf(x, dpp<0x128>(x)); return x;
}
FI float xrow_max(float x) {
  auto s = __builtin_amdgcn_permlane16_swap(__float_as_uint(x), __float_as_uint(x), false, false);
  x = fmaxf(__uint_as_float(s[0]), __uint_as_float(s[1]));
  auto t = __builtin_amdgcn_permlane32_swap(__float_as_uint(x), __float_as_uint(x), false, false);
  return fmaxf(__uint_as_float(t[0]), __uint_as_float(t[1]));
}
FI float xrow_sum(float x) {
  auto s = __builtin_amdgcn_permlane16_swap(__float_as_uint(x), __float_as_uint(x), false, false);
  x = __uint_as_float(s[0]) + __uint_as_float(s[1]);
  auto t = __builtin_amdgcn_permlane32_swap(__float_as_uint(x), __float_as_uint(x), false, false);
  return __uint_as_float(t[0]) + __uint_as_float(t[1]);
}
FI float wave_sum(float x) { return xrow_sum(row_sum(x)); }

FI void wg_barrier() { asm volatile("s_waitcnt lgkmcnt(0)\n\ts_barrier" ::: "memory"); }
template <int N> FI void wait_vm() { asm volatile("s_waitcnt vmcnt(%0)" ::"i"(N > 63 ? 63 : N) : "memory"); }

FI void mfma_acc(f4& d, const v4u& a, const v4u& b) {
  asm("s_nop 1\n\tv_mfma_f32_16x16x32_bf16 %0, %1, %2, %0" : "+v"(d) : "v"(a), "v"(b));
}
FI f4 mfma_first(const v4u& a, const v4u& b) {
  f4 d;
  asm("s_nop 1\n\tv_mfma_f32_16x16x32_bf16 %0, %1, %2, 0" : "=&v"(d) : "v"(a), "v"(b));
  return d;
}
FI void mfma_fence(f4& d) { asm("s_nop 7\n\ts_nop 1" : "+v"(d)); }

struct Masks { unsigned m0, m1, m2, m3; };
FI v4u diag(unsigned dup, const Masks& k) { v4u b = {dup & k.m0, dup & k.m1, dup & k.m2, dup & k.m3}; return b; }
FI unsigned dup_lo(unsigned x) { return __builtin_amdgcn_perm(x, x, 0x01000100u); }
FI unsigned dup_hi(unsigned x) { return __builtin_amdgcn_perm(x, x, 0x03020302u); }

struct Tile { v4u r[4]; };

struct Ln {
  int lane, g, c;
  Masks mk;
};
FI Ln make_ln() {
  Ln L;
  asm volatile("v_mbcnt_lo_u32_b32 %0, -1, 0\n\tv_mbcnt_hi_u32_b32 %0, -1, %0" : "=v"(L.lane));
  L.g = L.lane >> 4; L.c = L.lane & 15;
  const unsigned hm = (L.c & 1) ? 0xffff0000u : 0x0000ffffu;
  const int e2 = (L.c & 7) >> 1;
  L.mk.m0 = e2 == 0 ? hm : 0u; L.mk.m1 = e2 == 1 ? hm : 0u; L.mk.m2 = e2 == 2 ? hm : 0u; L.mk.m3 = e2 == 3 ? hm : 0u;
  return L;
}

FI void issue_k(unsigned m0v, unsigned voff, const v4i& rs, unsigned soff) {
  unsigned keep, tmp;
  asm volatile(
      "s_mov_b32 %0, m0\n\t"
      "s_mov_b32 m0, %2\n\t"
      "v_xor_b32 %1, 64, %3\n\t"
      "buffer_load_dwordx4 %3, %4, %5 offen nt lds\n\t"
      "buffer_load_dwordx4 %1, %4, %5 offen offset:1024 nt lds\n\t"
      "v_xor_b32 %1, 0x80, %3\n\t"
      "buffer_load_dwordx4 %1, %4, %5 offen offset:2048 nt lds\n\t"
      "v_xor_b32 %1, 0xc0, %3\n\t"
      "buffer_load_dwordx4 %1, %4, %5 offen offset:3072 nt lds\n\t"
      "s_mov_b32 m0, %0"
      : "=&s"(keep), "=&v"(tmp)
      : "s"(m0v), "v"(voff), "s"(rs), "s"(soff)
      : "memory");
}
#define V_LOADS \
  "buffer_load_dwordx4 %0, %4, %5, %6 offen nt\n\t" \
  "buffer_load_dwordx4 %1, %4, %5, %6 offen offset:1024 nt\n\t" \
  "buffer_load_dwordx4 %2, %4, %5, %6 offen offset:2048 nt\n\t" \
  "buffer_load_dwordx4 %3, %4, %5, %6 offen offset:3072 nt"
template <bool A> FI void issue_v_first(Tile& t, unsigned voff, const v4i& rs, unsigned soff) {
  if constexpr (A)
    asm volatile(V_LOADS : "=a"(t.r[0]), "=a"(t.r[1]), "=a"(t.r[2]), "=a"(t.r[3]) : "v"(voff), "s"(rs), "s"(soff) : "memory");
  else
    asm volatile(V_LOADS : "=&v"(t.r[0]), "=&v"(t.r[1]), "=&v"(t.r[2]), "=&v"(t.r[3]) : "v"(voff), "s"(rs), "s"(soff) : "memory");
}
FI void issue_v(Tile& t, unsigned voff, const v4i& rs, unsigned soff) {
  asm volatile(V_LOADS : "+a"(t.r[0]), "+a"(t.r[1]), "+a"(t.r[2]), "+a"(t.r[3]) : "v"(voff), "s"(rs), "s"(soff) : "memory");
}
template <bool A> FI void tile_def(Tile& t) {
  if constexpr (A) asm volatile("" : "=a"(t.r[0]), "=a"(t.r[1]), "=a"(t.r[2]), "=a"(t.r[3]));
  else asm volatile("" : "=v"(t.r[0]), "=v"(t.r[1]), "=v"(t.r[2]), "=v"(t.r[3]));
}
template <int SLOT> FI void read_k(Tile& k, unsigned addr) {
  unsigned tmp;
  asm volatile(
      "ds_read_b128 %0, %5 offset:%6\n\t"
      "v_xor_b32 %4, 16, %5\n\t"
      "ds_read_b128 %1, %4 offset:%6\n\t"
      "v_xor_b32 %4, 32, %5\n\t"
      "ds_read_b128 %2, %4 offset:%6\n\t"
      "v_xor_b32 %4, 48, %5\n\t"
      "ds_read_b128 %3, %4 offset:%6\n\t"
      "s_waitcnt lgkmcnt(0)"
      : "=&v"(k.r[0]), "=&v"(k.r[1]), "=&v"(k.r[2]), "=&v"(k.r[3]), "=&v"(tmp)
      : "v"(addr), "i"(SLOT * 4096)
      : "memory");
}
FI f4 qk(const Tile& k, const Tile& q) {
  f4 s;
  asm volatile(
      "v_mfma_f32_16x16x32_bf16 %0, %1, %5, 0\n\t"
      "v_mfma_f32_16x16x32_bf16 %0, %2, %6, %0\n\t"
      "v_mfma_f32_16x16x32_bf16 %0, %3, %7, %0\n\t"
      "v_mfma_f32_16x16x32_bf16 %0, %4, %8, %0\n\t"
      "s_nop 7\n\ts_nop 1"
      : "=&v"(s)
      : "v"(k.r[0]), "v"(k.r[1]), "v"(k.r[2]), "v"(k.r[3]), "v"(q.r[0]), "v"(q.r[1]), "v"(q.r[2]), "v"(q.r[3]));
  return s;
}
FI void load_b64(v2u& d, unsigned voff, const v4i& rs, unsigned soff) {
  asm volatile("buffer_load_dwordx2 %0, %1, %2, %3 offen" : "=v"(d) : "v"(voff), "s"(rs), "s"(soff) : "memory");
}
template <int N> FI void wait_wq(v2u (&q)[12]) {
  asm volatile("s_waitcnt vmcnt(%12)"
               : "+v"(q[0]), "+v"(q[1]), "+v"(q[2]), "+v"(q[3]), "+v"(q[4]), "+v"(q[5]), "+v"(q[6]), "+v"(q[7]), "+v"(q[8]), "+v"(q[9]),
                 "+v"(q[10]), "+v"(q[11])
               : "i"(N) : "memory");
}

struct Soft { f4 acc; float mc, l; };

FI f4 softmax_tile(Soft& st, const f4& s) {
  f4 t;
#pragma unroll
  for (int r = 0; r < 4; ++r) t[r] = __builtin_fmaf(s[r], QSCALE, -st.mc);
  const float tm = fmaxf(fmaxf(t[0], t[1]), fmaxf(t[2], t[3]));
  if (__builtin_amdgcn_ballot_w64(tm > 0.f) != 0ull) {
    const float sm = fmaxf(fmaxf(s[0], s[1]), fmaxf(s[2], s[3])) * QSCALE;
    const float mn = fmaxf(st.mc, xrow_max(sm));
    const float al = ex2(st.mc - mn);
    st.mc = mn;
    st.l *= al;
#pragma unroll
    for (int r = 0; r < 4; ++r) { st.acc[r] *= al; t[r] = __builtin_fmaf(s[r], QSCALE, -mn); }
  }
  f4 p;
#pragma unroll
  for (int r = 0; r < 4; ++r) p[r] = ex2(t[r]);
  st.l += (p[0] + p[1]) + (p[2] + p[3]);
  return p;
}
#define PV_MFMAS \
  "s_nop 1\n\t" \
  "v_mfma_f32_16x16x32_bf16 %0, %1, %5, %0\n\t" \
  "v_mfma_f32_16x16x32_bf16 %0, %2, %6, %0\n\t" \
  "v_mfma_f32_16x16x32_bf16 %0, %3, %7, %0\n\t" \
  "v_mfma_f32_16x16x32_bf16 %0, %4, %8, %0\n\t" \
  "s_nop 7\n\ts_nop 1"
template <bool A> FI void pv_tile(Soft& st, const f4& p, const Tile& v, const Masks& k) {
  const v4u b0 = diag(pk_bf(p[0], p[0]), k), b1 = diag(pk_bf(p[1], p[1]), k), b2 = diag(pk_bf(p[2], p[2]), k), b3 = diag(pk_bf(p[3], p[3]), k);
  if constexpr (A)
    asm volatile(PV_MFMAS : "+v"(st.acc) : "a"(v.r[0]), "a"(v.r[1]), "a"(v.r[2]), "a"(v.r[3]), "v"(b0), "v"(b1), "v"(b2), "v"(b3));
  else
    asm volatile(PV_MFMAS : "+v"(st.acc) : "v"(v.r[0]), "v"(v.r[1]), "v"(v.r[2]), "v"(v.r[3]), "v"(b0), "v"(b1), "v"(b2), "v"(b3));
}

constexpr bool vr_in_a(int i) { return i < 7; }

constexpr int imin(int a, int b) { return a < b ? a : b; }
constexpr int imax(int a, int b) { return a > b ? a : b; }
constexpr int issued1(int c) { return imin(2 * NTILE, imax(2 * B2, c + 1 + LA)); }
constexpr int slot1(int t) { return t < NR ? t : (t < T_RING ? RS + (t - NR) : (t - T_RING) % RS); }

#define POLL_BEGIN for (unsigned spins_ = 0;; ++spins_) { asm volatile("" ::: "memory");
#define POLL_END(ok) if (__builtin_amdgcn_ballot_w64(!(ok)) == 0ull || spins_ > SPIN_CAP) break; __builtin_amdgcn_s_sleep(1); }

constexpr int AUX_SC1 = 16;

#define STAMP(i) do {} while (0)

__global__ void __launch_bounds__(256, 1)
fused_decode_kernel(const unsigned short* __restrict__ xin, const unsigned char* __restrict__ kc, const unsigned char* __restrict__ vc,
                    const unsigned char* __restrict__ wq, const unsigned char* __restrict__ wo, const unsigned char* __restrict__ wg,
                    const unsigned char* __restrict__ wu, const unsigned char* __restrict__ wd, unsigned short* __restrict__ out,
                    unsigned char* ws) {
  __shared__ __attribute__((aligned(16))) unsigned char smem[L_BYTES];
  const int b = blockIdx.x;
  const int w = __builtin_amdgcn_readfirstlane(threadIdx.x >> 6);
  const int head = b >> 4, split = b & 15;
  const unsigned lbase = (unsigned)(size_t)smem;

  const v4i rsK = mk_rsrc(kc + ((size_t)b << 20), 1u << 20);
  const v4i rsV = mk_rsrc(vc + ((size_t)b << 20), 1u << 20);
  const v4i rsQ = mk_rsrc(wq, DM * NQ * 2);
  const __amdgpu_buffer_rsrc_t rsW = __builtin_amdgcn_make_buffer_rsrc(ws, 0, WS_BYTES, 0x00020000);

  const unsigned m0base = lbase + L_KS + w * (NL * 4096);
  const unsigned soffw = w * 4096;
  int lane, g, c, tid;
  Masks mk;
  unsigned voffV, voffK, addrK;
  auto relane = [&]() {
    const Ln L = make_ln();
    lane = L.lane; g = L.g; c = L.c; mk = L.mk; tid = w * 64 + lane;
    voffV = lane * 16;
    voffK = g * 256 + ((c ^ g) << 4);
    const unsigned kap = 4 * (c & 3) + (c >> 2);
    addrK = m0base + kap * 256 + (((4 * g) ^ kap) << 4);
  };
  relane();
  STAMP(0);

  Tile KR[NR], VR[NR], VL[NL];

  auto issue_group1 = [&](auto N) {
    constexpr int n = N.value, t = n >> 1;
    const unsigned so = soffw + t * 16384u;
    if constexpr ((n & 1) == 0) issue_k(m0base + slot1(t) * 4096, voffK, rsK, so);
    else if constexpr (t < NR) issue_v_first<vr_in_a(t)>(VR[t], voffV, rsV, so);
    else issue_v_first<true>(VL[slot1(t)], voffV, rsV, so);
  };

  v2u wqf[12];
  auto issue_wq = [&]() {
    const unsigned voffQ = (192u * w + (4 * g + (c >> 2)) * 12u) * (NQ * 2) + (unsigned)(b * 16 + 4 * (c & 3)) * 2u;
    sfor<0, 12>([&](auto I) {
      load_b64(wqf[I.value], voffQ, rsQ, I.value * NQ * 2);
    });
  };

  auto layernorm = [&](const v2u (&px)[3]) {
    float xv[3][4];
    const bool act2 = lane < 16;
#pragma unroll
    for (int k = 0; k < 3; ++k) {
      xv[k][0] = bf_lo(px[k][0]); xv[k][1] = bf_hi(px[k][0]); xv[k][2] = bf_lo(px[k][1]); xv[k][3] = bf_hi(px[k][1]);
    }
    float s = 0.f;
#pragma unroll
    for (int i = 0; i < 4; ++i) { if (!act2) xv[2][i] = 0.f; s += xv[0][i] + xv[1][i] + xv[2][i]; }
    const float mean = wave_sum(s) * (1.f / DM);
    float q = 0.f;
#pragma unroll
    for (int i = 0; i < 4; ++i) {
      const float a0 = xv[0][i] - mean, a1 = xv[1][i] - mean, a2 = act2 ? xv[2][i] - mean : 0.f;
      q += a0 * a0 + a1 * a1 + a2 * a2;
    }
    const float var = wave_sum(q) * (1.f / DM);
    const float mb = rbf(mean);
    const float den = rbf(__builtin_sqrtf(rbf(rbf(var) + 1e-5f)));
#pragma unroll
    for (int k = 0; k < 3; ++k) {
      v2u o;
      o[0] = pk_bf(rbf(xv[k][0] - mb) / den, rbf(xv[k][1] - mb) / den);
      o[1] = pk_bf(rbf(xv[k][2] - mb) / den, rbf(xv[k][3] - mb) / den);
      if (k < 2 || act2) *(v2u*)(smem + L_XB + (lane + 64 * k) * 8) = o;
    }
  };

  auto q_gemv = [&]() {
    if (w < 3) {
      const v2u* xp = (const v2u*)(smem + L_XB + (192 * w + (4 * g + (c >> 2)) * 12) * 2);
      const v2u xa = xp[0], xb = xp[1], xc = xp[2];
      const unsigned xs[6] = {xa[0], xa[1], xb[0], xb[1], xc[0], xc[1]};
      const unsigned ma = mk.m0 | mk.m2, mb = mk.m1 | mk.m3;
      f4 d;
#pragma unroll
      for (int n = 0; n < 6; ++n) {
        const unsigned lo = dup_lo(xs[n]), hi = dup_hi(xs[n]);
        const v4u bb = {lo & ma, lo & mb, hi & ma, hi & mb};
        const v4u aa = {wqf[2 * n][0], wqf[2 * n][1], wqf[2 * n + 1][0], wqf[2 * n + 1][1]};
        if (n == 0) d = mfma_first(aa, bb); else mfma_acc(d, aa, bb);
      }
      mfma_fence(d);
      if ((c >> 2) == g) {
        float* rp = (float*)(smem + L_REDA) + (w * 4 + g) * 16 + (c & 3);
#pragma unroll
        for (int r = 0; r < 4; ++r) rp[4 * r] = d[r];
      }
    }
  };
  auto q_exchange = [&](unsigned tag) {
    if (lane < 8) {
      const f2* rp = (const f2*)(smem + L_REDA) + lane;
      f2 a = rp[0];
#pragma unroll
      for (int i = 1; i < 12; ++i) a += rp[8 * i];
      v2u gr = {pk_bf(a[0], a[1]), tag};
      __builtin_amdgcn_raw_buffer_store_b64(gr, rsW, WS_QG + (b * 8 + lane) * 8, 0, AUX_SC1);
    }
    v4u v;
    POLL_BEGIN
      v = __builtin_amdgcn_raw_buffer_load_b128(rsW, WS_QG + head * 1024 + lane * 16, 0, AUX_SC1);
    POLL_END(v[1] == tag && v[3] == tag)
    v2u o = {v[0], v[2]};
    *(v2u*)(smem + L_QB + lane * 8) = o;
  };

  unsigned tagbase;
  {
    v2u px[3];
    unsigned ep;
    issue_wq();
    const v4i rsX = mk_rsrc(xin, DM * 2);
    const v4i rsE = mk_rsrc(ws, 64);
    asm volatile("buffer_load_dwordx2 %0, %4, %5, 0 offen\n\t"
                 "buffer_load_dwordx2 %1, %4, %5, 0 offen offset:512\n\t"
                 "buffer_load_dwordx2 %2, %4, %5, 0 offen offset:1024\n\t"
                 "buffer_load_dword %3, off, %6, 0 sc1"
                 : "=&v"(px[0]), "=&v"(px[1]), "=&v"(px[2]), "=&v"(ep)
                 : "v"(lane * 8), "s"(rsX), "s"(rsE) : "memory");
    sfor<0, 2 * B1>(issue_group1);
    asm volatile("s_waitcnt vmcnt(%16)"
                 : "+v"(px[0]), "+v"(px[1]), "+v"(px[2]), "+v"(ep), "+v"(wqf[0]), "+v"(wqf[1]), "+v"(wqf[2]), "+v"(wqf[3]), "+v"(wqf[4]),
                   "+v"(wqf[5]), "+v"(wqf[6]), "+v"(wqf[7]), "+v"(wqf[8]), "+v"(wqf[9]), "+v"(wqf[10]), "+v"(wqf[11])
                 : "i"(8 * B1) : "memory");
    tagbase = __builtin_amdgcn_readfirstlane(ep);
    if (w == 3) layernorm(px);
  }
  auto TAG = [&](unsigned ph) { return 0x80000000u | ((tagbase + ph) & 0x7fffffffu); };
  wg_barrier();
  q_gemv();
  wg_barrier();
  if (w == 3) q_exchange(TAG(1));
  sfor<2 * B1, 2 * B2>(issue_group1);
  wg_barrier();
  STAMP(1);
  {
    const v4u z = {0u, 0u, 0u, 0u};
    if (tid < 132) __builtin_amdgcn_raw_buffer_store_b128(z, rsW, WS_PA + b * PA_STRIDE + tid * 16, 0, AUX_SC1);
    __builtin_amdgcn_raw_buffer_store_b128(z, rsW, WS_PB + b * PB_STRIDE + tid * 16, 0, AUX_SC1);
    if (tid < 32) __builtin_amdgcn_raw_buffer_store_b128(z, rsW, WS_PB + b * PB_STRIDE + (256 + tid) * 16, 0, AUX_SC1);
    if (tid == 0 && b < NMLP) __builtin_amdgcn_raw_buffer_store_b128(z, rsW, WS_XG + b * 16, 0, AUX_SC1);
  }

  v2u xres;
  xres = *(const v2u*)(xin + (b < NMLP ? b : 0) * 4);

  Tile Q;
  Soft st;
  auto load_q = [&]() {
    const v4u* qp = (const v4u*)(smem + L_QB + (c >> 3) * 256 + g * 64);
#pragma unroll
    for (int j = 0; j < 4; ++j) Q.r[j] = qp[j];
    st.acc = (f4){0.f, 0.f, 0.f, 0.f};
    st.mc = -__builtin_inff();
    st.l = 0.f;
  };

  auto ring_step = [&](auto S, auto WK, auto WV, auto DOK, auto DOV, unsigned so_a, unsigned so_b) {
    constexpr int s = S.value;
    wait_vm<WK.value>();
    Tile kt;
    read_k<s>(kt, addrK);
    if constexpr (DOK.value) {
      if constexpr ((LA & 1) == 0) issue_k(m0base + ((s + LA / 2) % RS) * 4096, voffK, rsK, so_a);
      else issue_v(VL[(s + LA / 2) % RS], voffV, rsV, so_a);
    }
    const f4 sc = qk(kt, Q);
    const f4 p = softmax_tile(st, sc);
    wait_vm<WV.value>();
    pv_tile<true>(st, p, VL[s], mk);
    if constexpr (DOV.value) {
      if constexpr ((LA & 1) == 0) issue_v(VL[(s + LA / 2) % RS], voffV, rsV, so_b);
      else issue_k(m0base + ((s + (LA + 1) / 2) % RS) * 4096, voffK, rsK, so_b);
    }
  };
  using I0 = std::integral_constant<int, 0>;
  using I1 = std::integral_constant<int, 1>;
  using IW = std::integral_constant<int, 4 * (LA - 1)>;
  using I63 = std::integral_constant<int, 63>;

  auto chain = [&](auto LAYER) {
    constexpr int layer = LAYER.value;
    relane();
    STAMP(3 + 10 * layer);
    const unsigned ph = 2 + 5 * layer;

    v4u wof[4], woc[2];
    {
      const unsigned char* p = wo + (size_t)(b * 16 + 4 * g) * (DM * 2);
#pragma unroll
      for (int n = 0; n < 4; ++n) wof[n] = *(const v4u*)(p + n * (DM * 2) + 256 * w + 16 * c);
#pragma unroll
      for (int n = 0; n < 2; ++n) woc[n] = *(const v4u*)(p + (2 * n + (c >> 3)) * (DM * 2) + 1024 + 16 * (c & 7));
    }
    {
      const float lt = xrow_sum(st.l);
      float* cp = (float*)(smem + L_REDB) + w * 264;
#pragma unroll
      for (int r = 0; r < 4; ++r) cp[(c >> 3) * 128 + 32 * g + 8 * r + (c & 7)] = st.acc[r];
      if ((lane & 0x37) == 0) { f2 ml = {st.mc, lt}; *(f2*)(cp + 256 + 2 * (c >> 3)) = ml; }
    }
    wg_barrier();
    {
      const float* cp = (const float*)(smem + L_REDB);
      const int h = tid >> 7;
      f2 ml[4];
#pragma unroll
      for (int i = 0; i < 4; ++i) ml[i] = *(const f2*)(cp + i * 264 + 256 + 2 * h);
      const float M = fmaxf(fmaxf(ml[0][0], ml[1][0]), fmaxf(ml[2][0], ml[3][0]));
      float O = 0.f, L = 0.f;
#pragma unroll
      for (int i = 0; i < 4; ++i) { const float sc = ex2(ml[i][0] - M); O += sc * cp[i * 264 + tid]; L += sc * ml[i][1]; }
      const unsigned tg = TAG(ph);
      v2u gr = {__float_as_uint(O), tg};
      __builtin_amdgcn_raw_buffer_store_b64(gr, rsW, WS_PA + b * PA_STRIDE + tid * 8, 0, AUX_SC1);
      if ((tid & 127) == 0) {
        v4u g2 = {__float_as_uint(M), tg, __float_as_uint(L), tg};
        __builtin_amdgcn_raw_buffer_store_b128(g2, rsW, WS_PA + b * PA_STRIDE + (256 + 2 * h) * 8, 0, AUX_SC1);
      }
    }
    STAMP(4 + 10 * layer);
    {
      const unsigned tg = TAG(ph);
      const unsigned base = WS_PA + (head * 16 + c) * PA_STRIDE;
      v4u a0, a1, ml;
      POLL_BEGIN
        a0 = __builtin_amdgcn_raw_buffer_load_b128(rsW, base + (split * 16 + 4 * g) * 8, 0, AUX_SC1);
        a1 = __builtin_amdgcn_raw_buffer_load_b128(rsW, base + (split * 16 + 4 * g) * 8 + 16, 0, AUX_SC1);
        ml = __builtin_amdgcn_raw_buffer_load_b128(rsW, base + (256 + 2 * (split >> 3)) * 8, 0, AUX_SC1);
      POLL_END(a0[1] == tg && a0[3] == tg && a1[1] == tg && a1[3] == tg && ml[1] == tg && ml[3] == tg)
      STAMP(5 + 10 * layer);
      const float ms = __uint_as_float(ml[0]);
      const float wgt = ex2(ms - row_max(ms));
      const float inv = 1.f / row_sum(wgt * __uint_as_float(ml[2]));
      float o[4];
      o[0] = row_sum(wgt * __uint_as_float(a0[0])) * inv;
      o[1] = row_sum(wgt * __uint_as_float(a0[2])) * inv;
      o[2] = row_sum(wgt * __uint_as_float(a1[0])) * inv;
      o[3] = row_sum(wgt * __uint_as_float(a1[2])) * inv;
      f4 d = mfma_first(wof[0], diag(pk_bf(o[0], o[0]), mk));
#pragma unroll
      for (int n = 1; n < 4; ++n) mfma_acc(d, wof[n], diag(pk_bf(o[n], o[n]), mk));
      mfma_fence(d);
      const unsigned tb = TAG(ph + 1);
      const bool hp = (c >> 3) != 0;
      const unsigned ob = WS_PB + b * PB_STRIDE + (128 * w + 32 * g + (hp ? 16 : 0) + (c & 7)) * 8;
      v2u g0 = {__float_as_uint(hp ? d[2] : d[0]), tb}, g1 = {__float_as_uint(hp ? d[3] : d[1]), tb};
      __builtin_amdgcn_raw_buffer_store_b64(g0, rsW, ob, 0, AUX_SC1);
      __builtin_amdgcn_raw_buffer_store_b64(g1, rsW, ob + 64, 0, AUX_SC1);
      if (w == 0) {
        const float oa = hp ? o[1] : o[0], obv = hp ? o[3] : o[2];
        f4 e = mfma_first(woc[0], diag(pk_bf(oa, oa), mk));
        mfma_acc(e, woc[1], diag(pk_bf(obv, obv), mk));
        mfma_fence(e);
#pragma unroll
        for (int r = 0; r < 4; ++r) {
          const float sum = e[r] + __shfl(e[r], (lane + 40) & 63);
          if (g < 2 && !hp) {
            v2u gg = {__float_as_uint(sum), tb};
            __builtin_amdgcn_raw_buffer_store_b64(gg, rsW, WS_PB + b * PB_STRIDE + (512 + 8 * (4 * g + r) + (c & 7)) * 8, 0, AUX_SC1);
          }
        }
      }
    }
    STAMP(6 + 10 * layer);

    auto reduce_cols = [&](auto NPc, unsigned tg) {
      constexpr int NP = NPc.value, NK = (NP + 63) / 64;
      v4u a[NK][2];
      bool ok;
      POLL_BEGIN
        ok = true;
#pragma unroll
        for (int k = 0; k < NK; ++k) {
          const int pr = lane + 64 * k;
          const unsigned off = pr < NP ? WS_PB + pr * PB_STRIDE + b * 32 : 0x7fffff00u;
          a[k][0] = __builtin_amdgcn_raw_buffer_load_b128(rsW, off, 0, AUX_SC1);
          a[k][1] = __builtin_amdgcn_raw_buffer_load_b128(rsW, off + 16, 0, AUX_SC1);
        }
#pragma unroll
        for (int k = 0; k < NK; ++k) {
          const bool m = a[k][0][1] == tg && a[k][0][3] == tg && a[k][1][1] == tg && a[k][1][3] == tg;
          ok = ok && (m || lane + 64 * k >= NP);
        }
      POLL_END(ok)
      float s[4] = {0.f, 0.f, 0.f, 0.f};
#pragma unroll
      for (int k = 0; k < NK; ++k) {
        s[0] += __uint_as_float(a[k][0][0]); s[1] += __uint_as_float(a[k][0][2]);
        s[2] += __uint_as_float(a[k][1][0]); s[3] += __uint_as_float(a[k][1][2]);
      }
#pragma unroll
      for (int i = 0; i < 4; ++i) s[i] = rbf(wave_sum(s[i]));
      xres[0] = pk_bf(bf_lo(xres[0]) + s[0], bf_hi(xres[0]) + s[1]);
      xres[1] = pk_bf(bf_lo(xres[1]) + s[2], bf_hi(xres[1]) + s[3]);
    };
    auto publish_x = [&](unsigned tg) {
      if (lane == 0) {
        v4u gx = {xres[0], tg, xres[1], tg};
        __builtin_amdgcn_raw_buffer_store_b128(gx, rsW, WS_XG + b * 16, 0, AUX_SC1);
      }
    };
    auto gather_x_ln = [&](unsigned tg) {
      v4u a[3];
      POLL_BEGIN
#pragma unroll
        for (int k = 0; k < 3; ++k) {
          const int r = lane + 64 * k;
          a[k] = __builtin_amdgcn_raw_buffer_load_b128(rsW, r < NMLP ? WS_XG + r * 16 : 0x7fffff00u, 0, AUX_SC1);
        }
      POLL_END(a[0][1] == tg && a[0][3] == tg && a[1][1] == tg && a[1][3] == tg && ((a[2][1] == tg && a[2][3] == tg) || lane >= 16))
      v2u px[3];
#pragma unroll
      for (int k = 0; k < 3; ++k) { px[k][0] = a[k][0]; px[k][1] = a[k][2]; }
      layernorm(px);
    };

    if (b < NMLP) {
      if (w == 3) { reduce_cols(std::integral_constant<int, NWG>{}, TAG(ph + 1)); publish_x(TAG(ph + 2)); }
      STAMP(7 + 10 * layer);
      constexpr int GW = 6;
      v4u gu[GW];
      const unsigned char* pgu = (((c >> 2) & 1) ? wu : wg) + (size_t)(144 * w + (2 * g + (c >> 3)) * 18) * (DFF * 2) + 64 * b + 16 * (c & 3);
#pragma unroll
      for (int n = 0; n < GW; ++n) gu[n] = *(const v4u*)(pgu + (size_t)n * (DFF * 2));
      if (w == 3) gather_x_ln(TAG(ph + 2));
      wg_barrier();
      STAMP(8 + 10 * layer);
      f4 d;
      {
        const unsigned* xp = (const unsigned*)(smem + L_XB + (144 * w + (2 * g + (c >> 3)) * 18) * 2);
        unsigned xs[9];
#pragma unroll
        for (int n = 0; n < 9; ++n) xs[n] = xp[n];
#pragma unroll
        for (int n = 0; n < 18; ++n) {
          const v4u bb = diag((n & 1) ? dup_hi(xs[n >> 1]) : dup_lo(xs[n >> 1]), mk);
          if (n == 0) d = mfma_first(gu[0], bb); else mfma_acc(d, gu[n % GW], bb);
          if (n + GW < 18) gu[n % GW] = *(const v4u*)(pgu + (size_t)(n + GW) * (DFF * 2));
          __builtin_amdgcn_sched_barrier(0);
        }
      }
      v4u dn[9];
      {
        const unsigned char* p = wd + (size_t)(32 * b + 8 * w + 2 * g) * (DM * 2);
#pragma unroll
        for (int bl = 0; bl < 4; ++bl) {
          dn[2 * bl] = *(const v4u*)(p + 256 * bl + 16 * c);
          dn[2 * bl + 1] = *(const v4u*)(p + DM * 2 + 256 * bl + 16 * c);
        }
        dn[8] = *(const v4u*)(p + (c >> 3) * (DM * 2) + 1024 + 16 * (c & 7));
      }
      mfma_fence(d);
      if ((g >> 1) == (c >> 3)) {
        float* rp = (float*)(smem + L_REDA) + (w * 4 + g) * 32 + (c & 7);
#pragma unroll
        for (int r = 0; r < 4; ++r) rp[8 * r] = d[r];
      }
      wg_barrier();
      unsigned hd0, hd1;
      {
        const bool hp = (c >> 3) != 0;
        f2 v = ((const f2*)(smem + L_REDA))[4 * w + g + 32 * (c & 7) + 16 * (c >> 3)];
        f2 ga, ua;
#pragma unroll
        for (int i = 0; i < 2; ++i) {
          float x = v[i];
          x += dpp<0xB1>(x); x += dpp<0x4E>(x); x += dpp<0x141>(x);
          const float y = dpp<0x128>(x);
          ga[i] = hp ? y : x; ua[i] = hp ? x : y;
        }
        float hv[2];
#pragma unroll
        for (int i = 0; i < 2; ++i) {
          const float x = rbf(ga[i]);
          const float z = 0.7978845608028654f * (x + 0.044715f * x * x * x);
          const float th = 1.f - 2.f / (__expf(2.f * z) + 1.f);
          hv[i] = rbf(0.5f * x * (1.f + th)) * rbf(ua[i]);
        }
        hd0 = pk_bf(hv[0], hv[0]); hd1 = pk_bf(hv[1], hv[1]);
      }
      {
        const bool hp = (c >> 3) != 0;
        const v4u b0 = diag(hd0, mk), b1 = diag(hd1, mk);
        float* rp = (float*)(smem + L_REDB);
#pragma unroll
        for (int bl = 0; bl < 4; ++bl) {
          f4 e = mfma_first(dn[2 * bl], b0);
          mfma_acc(e, dn[2 * bl + 1], b1);
          mfma_fence(e);
          float* q = rp + w * 512 + 128 * bl + 32 * g + (hp ? 16 : 0) + (c & 7);
          q[0] = hp ? e[2] : e[0]; q[8] = hp ? e[3] : e[1];
        }
        f4 e = mfma_first(dn[8], diag(hp ? hd1 : hd0, mk));
        mfma_fence(e);
        if ((g >> 1) == (c >> 3)) {
          float* q = rp + 2048 + w * 128 + 32 * g + (c & 7);
#pragma unroll
          for (int r = 0; r < 4; ++r) q[8 * r] = e[r];
        }
      }
      if constexpr (layer == 0) issue_wq();
      wg_barrier();
      if (tid < NMLP) {
        const f4* rp = (const f4*)(smem + L_REDB);
        f4 a;
        if (tid < 128) a = (rp[tid] + rp[128 + tid]) + (rp[256 + tid] + rp[384 + tid]);
        else {
          a = rp[512 + tid - 128];
#pragma unroll
          for (int i = 1; i < 8; ++i) a += rp[512 + 16 * i + tid - 128];
        }
        const unsigned tg = TAG(ph + 3);
        v4u g0 = {__float_as_uint(a[0]), tg, __float_as_uint(a[1]), tg}, g1 = {__float_as_uint(a[2]), tg, __float_as_uint(a[3]), tg};
        __builtin_amdgcn_raw_buffer_store_b128(g0, rsW, WS_PB + b * PB_STRIDE + tid * 32, 0, AUX_SC1);
        __builtin_amdgcn_raw_buffer_store_b128(g1, rsW, WS_PB + b * PB_STRIDE + tid * 32 + 16, 0, AUX_SC1);
      }
      STAMP(9 + 10 * layer);
      if (w == 3) {
        reduce_cols(std::integral_constant<int, NMLP>{}, TAG(ph + 3));
        if constexpr (layer == 0) publish_x(TAG(ph + 4));
        else if (lane == 0) {
          *(v2u*)(out + 4 * b) = xres;
          if (b == 0) __builtin_amdgcn_raw_buffer_store_b32(tagbase + 16u, rsW, WS_EPOCH, 0, AUX_SC1);
        }
      }
      STAMP(10 + 10 * layer);
    } else if constexpr (layer == 0) {
      issue_wq();
    }
    if constexpr (layer == 0) {
      if (w == 3) gather_x_ln(TAG(ph + 4));
      wg_barrier();
      wait_wq<0>(wqf);
      q_gemv();
      wg_barrier();
      if (w == 3) q_exchange(TAG(ph + 5));
      wg_barrier();
      STAMP(11);
    }
  };

  load_q();
  {
      sfor<0, T_RING>([&](auto T) {
        constexpr int t = T.value;
        wait_vm<4 * (issued1(2 * t - 1) - (2 * t + 1))>();
        Tile kt;
        if constexpr (t < NR) read_k<slot1(t)>(KR[t], addrK); else read_k<slot1(t)>(kt, addrK);
        sfor<issued1(2 * t - 1), issued1(2 * t)>(issue_group1);
        f4 sc;
        if constexpr (t < NR) sc = qk(KR[t], Q); else sc = qk(kt, Q);
        const f4 p = softmax_tile(st, sc);
        wait_vm<4 * (issued1(2 * t) - (2 * t + 2))>();
        if constexpr (t < NR) pv_tile<vr_in_a(t)>(st, p, VR[t], mk); else pv_tile<true>(st, p, VL[slot1(t)], mk);
        sfor<issued1(2 * t), issued1(2 * t + 1)>(issue_group1);
      });
      STAMP(2);
      if constexpr (LA == 3) tile_def<true>(VL[1]);
#pragma unroll 1
      for (int t = T_RING; t < NTILE - RS; t += RS) {
        sfor<0, RS>([&](auto S) {
          const unsigned tt = t + S.value;
          ring_step(S, IW{}, IW{}, I1{}, I1{}, soffw + (tt + LA / 2) * 16384u, soffw + (tt + (LA + 1) / 2) * 16384u);
        });
      }
      sfor<0, RS>([&](auto S) {
        constexpr int t = NTILE - RS + S.value;
        ring_step(S, std::integral_constant<int, 4 * (issued1(2 * t - 1) - (2 * t + 1))>{},
                  std::integral_constant<int, 4 * (issued1(2 * t) - (2 * t + 2))>{},
                  std::integral_constant<int, (issued1(2 * t) > issued1(2 * t - 1))>{},
                  std::integral_constant<int, (issued1(2 * t + 1) > issued1(2 * t))>{},
                  soffw + (t + LA / 2) * 16384u, soffw + (t + (LA + 1) / 2) * 16384u);
      });
  }
  chain(I0{});
  relane();
  load_q();
  {
      constexpr int NKEEP = NR + NL - RS;
      constexpr int NU = (NKEEP + RS - 1) / RS * RS;
      sfor<0, NU>([&](auto U) {
        constexpr int u = U.value - RS;
        constexpr int s = U.value % RS;
        constexpr int ga = 2 * u + LA, gb = 2 * u + 1 + LA;
        constexpr int wk = u < 0 ? 63 : 4 * (LA - 1), wv = u < 0 ? 63 : 4 * (LA - 1);
        ring_step(std::integral_constant<int, s>{}, std::integral_constant<int, wk>{}, std::integral_constant<int, wv>{},
                  std::integral_constant<int, (ga >= 0)>{}, std::integral_constant<int, (gb >= 0)>{},
                  soffw + (T_RING + u + LA / 2) * 16384u, soffw + (T_RING + u + (LA + 1) / 2) * 16384u);
        if constexpr (U.value < NKEEP) {
          constexpr int i = U.value;
          if constexpr (i < NR) {
            const f4 p = softmax_tile(st, qk(KR[i], Q));
            pv_tile<vr_in_a(i)>(st, p, VR[i], mk);
          } else {
            Tile kt;
            read_k<RS + (i - NR)>(kt, addrK);
            const f4 p = softmax_tile(st, qk(kt, Q));
            pv_tile<true>(st, p, VL[RS + (i - NR)], mk);
          }
        }
      });
      STAMP(12);
#pragma unroll 1
      for (int u = NU - RS; u < NSTREAM - RS; u += RS) {
        sfor<0, RS>([&](auto S) {
          const unsigned tt = T_RING + u + S.value;
          ring_step(S, IW{}, IW{}, I1{}, I1{}, soffw + (tt + LA / 2) * 16384u, soffw + (tt + (LA + 1) / 2) * 16384u);
        });
      }
      sfor<0, RS>([&](auto S) {
        constexpr int t = NTILE - RS + S.value;
        ring_step(S, std::integral_constant<int, 4 * (issued1(2 * t - 1) - (2 * t + 1))>{},
                  std::integral_constant<int, 4 * (issued1(2 * t) - (2 * t + 2))>{},
                  std::integral_constant<int, (issued1(2 * t) > issued1(2 * t - 1))>{},
                  std::integral_constant<int, (issued1(2 * t + 1) > issued1(2 * t))>{},
                  soffw + (t - RS + LA / 2) * 16384u, soffw + (t - RS + (LA + 1) / 2) * 16384u);
      });
  }
  chain(I1{});
}

}

extern "C" void kernel_launch(void* const* d_in, const int* in_sizes, int n_in, void* d_out, int out_size, void* d_ws, size_t ws_size,
                              hipStream_t stream) {
  (void)in_sizes; (void)n_in; (void)out_size; (void)ws_size;
  fused_decode_kernel<<<NWG, 256, 0, stream>>>(
      (const unsigned short*)d_in[0], (const unsigned char*)d_in[1], (const unsigned char*)d_in[2], (const unsigned char*)d_in[3],
      (const unsigned char*)d_in[4], (const unsigned char*)d_in[6], (const unsigned char*)d_in[7], (const unsigned char*)d_in[8],
      (unsigned short*)d_out, (unsigned char*)d_ws);
}
```

```cpp
#define GATE_WIN 9
#include <hip/hip_runtime.h>
#include <hip/hip_bf16.h>
#include <type_traits>
#include <cstdio>

#define DIAG 0

#define QSMAP 0
#define MUMAP 0
#define EARLY_F0 0
#define EARLY_F1 0
#define WO_EARLY 3
#define DELAY_PA 6
#define DELAY_PB 12
#define DELAY_Q 6
#define CODE_WARM 1
#define PARK_AT 2
#define PARK_STEP 3
#define PARK_PACE 1600
#define WARM1 3
#define WARM2 6
#define WARM_WAIT 0
#define GRADED 1
#define LAG1 30
#define LAG2 150
#define LAG3 400
#define NPARK 0
#define WDEF 7
namespace {

typedef unsigned v4u __attribute__((ext_vector_type(4)));
typedef unsigned v2u __attribute__((ext_vector_type(2)));
typedef int v4i __attribute__((ext_vector_type(4)));
typedef float f4 __attribute__((ext_vector_type(4)));
typedef float f2 __attribute__((ext_vector_type(2)));
typedef __bf16 bf2 __attribute__((ext_vector_type(2)));
#define GAS __attribute__((address_space(1)))
#define FI __device__ __forceinline__

constexpr int DM = 576, DFF = 4608, NQ = 4096;
constexpr int NWG = 256;
constexpr int NMLP = 144;
constexpr int NRED = 36;
constexpr int XREP = 8;
constexpr int NTILE = 64;
constexpr int NR = 9;
constexpr int NL = 9;
#define DLAYER 0
#define INFLIGHT 8
constexpr int UF = INFLIGHT;
constexpr int RS = 2;
constexpr int NSTREAM = NTILE - NR - NL;
#define BURST2 6
#define UFPRE 8
#define RAMP8 3
#define PRE1 20
constexpr int P1 = PRE1;
constexpr int B2 = BURST2;
constexpr int T_RING = NR + NL - RS;
static_assert(GATE_WIN >= 9, "all down fragments must fit the window");
static_assert(PRE1 >= 8 + UFPRE, "the wq wait counts on UFPRE younger loads");
static_assert(UF == 8 || WO_EARLY == 0, "the last tile has been requested in full before the wo loads go out");
static_assert(UF >= 2 && UF <= 8 && B2 <= NR, "schedule");
#define PACE 3552
#define ROT_EXPR 0
#define PACE1 3584
#define PACE_EARLY 3712
#define PACE_OFF1 200
#define PACE_OFF2 30
#define PACE_MASK 3
constexpr int NKEEP = NR + NL - RS;
constexpr float QSCALE = 0.08838834764831845f * 1.4426950408889634f;
constexpr unsigned SPIN_CAP = 200000u;

constexpr unsigned WS_EPOCH = 0;
constexpr unsigned WS_QG = 4096;
constexpr unsigned WS_XG = 32768, XG_STRIDE = 4096;
constexpr unsigned WS_PA = 65536 + 32768, PA_MSG = 160, PA_STRIDE = 16 * PA_MSG;
constexpr unsigned WS_PB = 1u << 20, PB_STRIDE = NWG * 128;
constexpr unsigned WS_BYTES = WS_PB + NRED * PB_STRIDE;
constexpr unsigned OOB = 0x7fffff00u;
constexpr unsigned WS_DIAG = 4u << 20;

constexpr int L_KS = 0;
constexpr int L_SC = 4 * NL * 4096;
constexpr int L_REDB = L_SC;
constexpr int L_REDA = L_SC + 10240;
constexpr int L_XB = L_REDA + 2048;
constexpr int L_QB = L_XB + 1280;
constexpr int L_PAD = L_QB + 512;
constexpr int L_FLAG = L_PAD + 1024;
constexpr int L_XR = L_FLAG + 32;
constexpr int L_BYTES = L_XR + 32;
static_assert(L_BYTES <= 163840, "LDS");

template <int B, int E, class F> FI void sfor(F&& f) {
  if constexpr (B < E) { f(std::integral_constant<int, B>{}); sfor<B + 1, E>(f); }
}

FI v4i mk_rsrc(const void* p, unsigned bytes) {
  unsigned long long a = (unsigned long long)p;
  v4i r = {(int)(unsigned)a, (int)((unsigned)(a >> 32) & 0xffffu), (int)bytes, 0x00020000};
  return r;
}
FI float bf_lo(unsigned u) { return __uint_as_float(u << 16); }
FI float bf_hi(unsigned u) { return __uint_as_float(u & 0xffff0000u); }
FI unsigned pk_bf(float a, float b) { f2 v = {a, b}; return __builtin_bit_cast(unsigned, __builtin_convertvector(v, bf2)); }
FI float rbf(float a) { return bf_lo(pk_bf(a, a)); }
FI float frcp(float x) { const float r = __builtin_amdgcn_rcpf(x); return __builtin_fmaf(__builtin_fmaf(-x, r, 1.f), r, r); }
FI float ex2(float x) { return __builtin_amdgcn_exp2f(x); }

template <int CTRL> FI float dpp(float x) {
  return __builtin_bit_cast(float, __builtin_amdgcn_mov_dpp(__builtin_bit_cast(int, x), CTRL, 0xf, 0xf, true));
}
FI float row_sum(float x) {
  x += dpp<0xB1>(x); x += dpp<0x4E>(x); x += dpp<0x141>(x); x += dpp<0x128>(x); return x;
}
FI float row_max(float x) {
  x = fmaxf(x, dpp<0xB1>(x)); x = fmaxf(x, dpp<0x4E>(x)); x = fmaxf(x, dpp<0x141>(x)); x = fmaxf(x, dpp<0x128>(x)); return x;
}
FI float xrow_max(float x) {
  auto s = __builtin_amdgcn_permlane16_swap(__float_as_uint(x), __float_as_uint(x), false, false);
  x = fmaxf(__uint_as_float(s[0]), __uint_as_float(s[1]));
  auto t = __builtin_amdgcn_permlane32_swap(__float_as_uint(x), __float_as_uint(x), false, false);
  return fmaxf(__uint_as_float(t[0]), __uint_as_float(t[1]));
}
FI float xrow_sum(float x) {
  auto s = __builtin_amdgcn_permlane16_swap(__float_as_uint(x), __float_as_uint(x), false, false);
  x = __uint_as_float(s[0]) + __uint_as_float(s[1]);
  auto t = __builtin_amdgcn_permlane32_swap(__float_as_uint(x), __float_as_uint(x), false, false);
  return __uint_as_float(t[0]) + __uint_as_float(t[1]);
}
FI float wave_sum(float x) { return xrow_sum(row_sum(x)); }

FI void wg_barrier() { asm volatile("s_waitcnt lgkmcnt(0)\n\ts_barrier" ::: "memory"); }
template <int N> FI void wait_vm() { asm volatile("s_waitcnt vmcnt(%0)" ::"i"(N > 63 ? 63 : N) : "memory"); }

template <int N> FI void wait_issue(int lag) {
  if constexpr (!GRADED || N != 7) wait_vm<N>();
  else
    asm volatile(
        "s_cmp_gt_i32 %0, %3\n\ts_cbranch_scc1 1f\n\ts_waitcnt vmcnt(6)\n\t"
        "s_cmp_gt_i32 %0, %2\n\ts_cbranch_scc1 1f\n\ts_waitcnt vmcnt(5)\n\t"
        "s_cmp_gt_i32 %0, %1\n\ts_cbranch_scc1 1f\n\ts_waitcnt vmcnt(4)\n"
        "1:\n\ts_waitcnt vmcnt(7)"
        ::"s"(lag), "i"(LAG1 * 256), "i"(LAG2 * 256), "i"(LAG3 * 256) : "memory", "scc");
}

FI void mfma_acc(f4& d, const v4u& a, const v4u& b) {
  asm("s_nop 1\n\tv_mfma_f32_16x16x32_bf16 %0, %1, %2, %0" : "+v"(d) : "v"(a), "v"(b));
}
FI f4 mfma_first(const v4u& a, const v4u& b) {
  f4 d;
  asm("s_nop 1\n\tv_mfma_f32_16x16x32_bf16 %0, %1, %2, 0" : "=&v"(d) : "v"(a), "v"(b));
  return d;
}
FI void mfma_fence(f4& d) { asm("s_nop 7\n\ts_nop 1" : "+v"(d)); }

FI void mfma_fence5(f4 (&e)[5]) { asm("s_nop 7\n\ts_nop 1" : "+v"(e[0]), "+v"(e[1]), "+v"(e[2]), "+v"(e[3]), "+v"(e[4])); }

struct Masks { unsigned m0, m1, m2, m3; };
FI v4u diag(unsigned dup, const Masks& k) { v4u b = {dup & k.m0, dup & k.m1, dup & k.m2, dup & k.m3}; return b; }
FI unsigned dup_lo(unsigned x) { return __builtin_amdgcn_perm(x, x, 0x01000100u); }
FI unsigned dup_hi(unsigned x) { return __builtin_amdgcn_perm(x, x, 0x03020302u); }

struct Tile { v4u r[4]; };

struct Ln {
  int lane, g, c;
  Masks mk;
};
FI Ln make_ln() {
  Ln L;
  asm volatile("v_mbcnt_lo_u32_b32 %0, -1, 0\n\tv_mbcnt_hi_u32_b32 %0, -1, %0" : "=v"(L.lane));
  L.g = L.lane >> 4; L.c = L.lane & 15;
  const unsigned hm = (L.c & 1) ? 0xffff0000u : 0x0000ffffu;
  const int e2 = (L.c & 7) >> 1;
  L.mk.m0 = e2 == 0 ? hm : 0u; L.mk.m1 = e2 == 1 ? hm : 0u; L.mk.m2 = e2 == 2 ? hm : 0u; L.mk.m3 = e2 == 3 ? hm : 0u;
  return L;
}

#define K_LOAD(POL) \
  "s_mov_b32 %0, m0\n\t" \
  "s_mov_b32 m0, %2\n\t" \
  "v_xor_b32 %1, %6, %3\n\t" \
  "buffer_load_dwordx4 %1, %4, %5 offen offset:%7 " POL " lds\n\t" \
  "s_mov_b32 m0, %0"
template <int I, bool KEEP = false> FI void issue_k(unsigned m0v, unsigned voff, const v4i& rs, unsigned soff) {
  unsigned keep, tmp;
  if constexpr (KEEP)
    asm volatile(K_LOAD("") : "=&s"(keep), "=&v"(tmp) : "s"(m0v), "v"(voff), "s"(rs), "s"(soff), "i"(64 * I), "i"(1024 * I) : "memory");
  else
    asm volatile(K_LOAD("nt") : "=&s"(keep), "=&v"(tmp) : "s"(m0v), "v"(voff), "s"(rs), "s"(soff), "i"(64 * I), "i"(1024 * I) : "memory");
}
template <int I, int MODE> FI void issue_v(v4u& t, unsigned voff, const v4i& rs, unsigned soff) {
  if constexpr (MODE == 0)
    asm volatile("buffer_load_dwordx4 %0, %1, %2, %3 offen offset:%4 nt" : "=a"(t) : "v"(voff), "s"(rs), "s"(soff), "i"(1024 * I) : "memory");
  else if constexpr (MODE == 1)
    asm volatile("buffer_load_dwordx4 %0, %1, %2, %3 offen offset:%4 nt" : "=&v"(t) : "v"(voff), "s"(rs), "s"(soff), "i"(1024 * I) : "memory");
  else if constexpr (MODE == 2)
    asm volatile("buffer_load_dwordx4 %0, %1, %2, %3 offen offset:%4 nt" ::"a"(t), "v"(voff), "s"(rs), "s"(soff), "i"(1024 * I) : "memory");
  else
    asm volatile("buffer_load_dwordx4 %0, %1, %2, %3 offen offset:%4" ::"a"(t), "v"(voff), "s"(rs), "s"(soff), "i"(1024 * I) : "memory");
}
FI void def_a(v4u& t) { asm volatile("" : "=a"(t)); }
FI void touch(unsigned m0v, unsigned voff, const v4i& rs, unsigned soff) {
  unsigned keep;
  asm volatile(
      "s_mov_b32 %0, m0\n\t"
      "s_mov_b32 m0, %1\n\t"
      "s_nop 0\n\t"
      "buffer_load_dword %2, %3, %4 offen lds\n\t"
      "s_mov_b32 m0, %0"
      : "=&s"(keep)
      : "s"(m0v), "v"(voff), "s"(rs), "s"(soff)
      : "memory");
}
FI void touch16(unsigned m0v, unsigned voff, const v4i& rs, unsigned soff) {
  unsigned keep;
  asm volatile(
      "s_mov_b32 %0, m0\n\t"
      "s_mov_b32 m0, %1\n\t"
      "s_nop 0\n\t"
      "buffer_load_dwordx4 %2, %3, %4 offen lds\n\t"
      "s_mov_b32 m0, %0"
      : "=&s"(keep)
      : "s"(m0v), "v"(voff), "s"(rs), "s"(soff)
      : "memory");
}
FI void opaque3(v2u (&p)[3]) { asm volatile("" : "+v"(p[0]), "+v"(p[1]), "+v"(p[2])); }
FI void ldc1(v4u& a, const v4i& rs, unsigned v0) {
  asm volatile("buffer_load_dwordx4 %0, %2, %1, 0 offen sc1\n\ts_waitcnt vmcnt(0)" : "+v"(a) : "s"(rs), "v"(v0) : "memory");
}
template <int IMM> FI void ldc3(v4u (&a)[3], const v4i& rs, unsigned v0, unsigned v2) {
  asm volatile(
      "buffer_load_dwordx4 %0, %4, %3, 0 offen sc1\n\t"
      "buffer_load_dwordx4 %1, %4, %3, 0 offen offset:%6 sc1\n\t"
      "buffer_load_dwordx4 %2, %5, %3, 0 offen sc1\n\t"
      "s_waitcnt vmcnt(0)"
      : "+v"(a[0]), "+v"(a[1]), "+v"(a[2]) : "s"(rs), "v"(v0), "v"(v2), "i"(IMM) : "memory");
}
FI void ldc4(v4u& a0, v4u& a1, v4u& a2, v4u& a3, const v4i& rs, unsigned v0, unsigned so) {
  asm volatile(
      "buffer_load_dwordx4 %0, %5, %4, %6 offen sc1\n\t"
      "buffer_load_dwordx4 %1, %5, %4, %7 offen sc1\n\t"
      "buffer_load_dwordx4 %2, %5, %4, %8 offen sc1\n\t"
      "buffer_load_dwordx4 %3, %5, %4, %9 offen sc1"
      : "+v"(a0), "+v"(a1), "+v"(a2), "+v"(a3) : "s"(rs), "v"(v0), "s"(so), "s"(so + 4096u), "s"(so + 8192u), "s"(so + 12288u) : "memory");
}
FI void pin4(v4u& a0, v4u& a1, v4u& a2, v4u& a3) { asm volatile("s_waitcnt vmcnt(0)" : "+v"(a0), "+v"(a1), "+v"(a2), "+v"(a3) :: "memory"); }
FI bool tagged(const v4u& a, unsigned tg) { return a[1] == tg && a[3] == tg; }
FI void lds_put(unsigned addr, unsigned v) { asm volatile("s_waitcnt lgkmcnt(0)\n\tds_write_b32 %0, %1" ::"v"(addr), "v"(v) : "memory"); }
FI unsigned lds_get(unsigned addr) {
  unsigned v;
  asm volatile("ds_read_b32 %0, %1\n\ts_waitcnt lgkmcnt(0)" : "=v"(v) : "v"(addr) : "memory");
  return __builtin_amdgcn_readfirstlane(v);
}
template <bool DEF> FI v4u ld_w(const unsigned char* p) { if constexpr (DEF) return *(const v4u*)p; else return __builtin_nontemporal_load((const v4u*)p); }
template <int SLOT> FI void read_k(Tile& k, unsigned addr) {
  unsigned tmp;
  asm volatile(
      "ds_read_b128 %0, %5 offset:%6\n\t"
      "v_xor_b32 %4, 16, %5\n\t"
      "ds_read_b128 %1, %4 offset:%6\n\t"
      "v_xor_b32 %4, 32, %5\n\t"
      "ds_read_b128 %2, %4 offset:%6\n\t"
      "v_xor_b32 %4, 48, %5\n\t"
      "ds_read_b128 %3, %4 offset:%6\n\t"
      "s_waitcnt lgkmcnt(0)"
      : "=&v"(k.r[0]), "=&v"(k.r[1]), "=&v"(k.r[2]), "=&v"(k.r[3]), "=&v"(tmp)
      : "v"(addr), "i"(SLOT * 4096)
      : "memory");
}
FI f4 qk(const Tile& k, const Tile& q) {
  f4 s;
  asm volatile(
      "v_mfma_f32_16x16x32_bf16 %0, %1, %5, 0\n\t"
      "v_mfma_f32_16x16x32_bf16 %0, %2, %6, %0\n\t"
      "v_mfma_f32_16x16x32_bf16 %0, %3, %7, %0\n\t"
      "v_mfma_f32_16x16x32_bf16 %0, %4, %8, %0\n\t"
      "s_nop 7\n\ts_nop 1"
      : "=&v"(s)
      : "v"(k.r[0]), "v"(k.r[1]), "v"(k.r[2]), "v"(k.r[3]), "v"(q.r[0]), "v"(q.r[1]), "v"(q.r[2]), "v"(q.r[3]));
  return s;
}
FI void load_b64(v2u& d, unsigned voff, const v4i& rs, unsigned soff) {
  asm volatile("buffer_load_dwordx2 %0, %1, %2, %3 offen nt" : "=v"(d) : "v"(voff), "s"(rs), "s"(soff) : "memory");
}
template <int N> FI void wait_wq(v2u (&q)[12]) {
  asm volatile("s_waitcnt vmcnt(%12)"
               : "+v"(q[0]), "+v"(q[1]), "+v"(q[2]), "+v"(q[3]), "+v"(q[4]), "+v"(q[5]), "+v"(q[6]), "+v"(q[7]), "+v"(q[8]), "+v"(q[9]),
                 "+v"(q[10]), "+v"(q[11])
               : "i"(N) : "memory");
}

struct Soft { f4 acc; float mc, l; };

FI f4 softmax_tile(Soft& st, const f4& s) {
  f4 t;
#pragma unroll
  for (int r = 0; r < 4; ++r) t[r] = __builtin_fmaf(s[r], QSCALE, -st.mc);
  const float tm = fmaxf(fmaxf(t[0], t[1]), fmaxf(t[2], t[3]));
  if (__builtin_amdgcn_ballot_w64(tm > 0.f) != 0ull) {
    const float sm = fmaxf(fmaxf(s[0], s[1]), fmaxf(s[2], s[3])) * QSCALE;
    const float mn = fmaxf(st.mc, xrow_max(sm));
    const float al = ex2(st.mc - mn);
    st.mc = mn;
    st.l *= al;
#pragma unroll
    for (int r = 0; r < 4; ++r) { st.acc[r] *= al; t[r] = __builtin_fmaf(s[r], QSCALE, -mn); }
  }
  f4 p;
#pragma unroll
  for (int r = 0; r < 4; ++r) p[r] = ex2(t[r]);
  st.l += (p[0] + p[1]) + (p[2] + p[3]);
  return p;
}
#define PV_MFMAS \
  "s_nop 1\n\t" \
  "v_mfma_f32_16x16x32_bf16 %0, %1, %5, %0\n\t" \
  "v_mfma_f32_16x16x32_bf16 %0, %2, %6, %0\n\t" \
  "v_mfma_f32_16x16x32_bf16 %0, %3, %7, %0\n\t" \
  "v_mfma_f32_16x16x32_bf16 %0, %4, %8, %0\n\t" \
  "s_nop 7\n\ts_nop 1"
template <bool A> FI void pv_tile(Soft& st, const f4& p, const Tile& v, const Masks& k) {
  const v4u b0 = diag(pk_bf(p[0], p[0]), k), b1 = diag(pk_bf(p[1], p[1]), k), b2 = diag(pk_bf(p[2], p[2]), k), b3 = diag(pk_bf(p[3], p[3]), k);
  if constexpr (A)
    asm volatile(PV_MFMAS : "+v"(st.acc) : "a"(v.r[0]), "a"(v.r[1]), "a"(v.r[2]), "a"(v.r[3]), "v"(b0), "v"(b1), "v"(b2), "v"(b3));
  else
    asm volatile(PV_MFMAS : "+v"(st.acc) : "v"(v.r[0]), "v"(v.r[1]), "v"(v.r[2]), "v"(v.r[3]), "v"(b0), "v"(b1), "v"(b2), "v"(b3));
}

constexpr bool vr_in_a(int i) { return i < 7; }

constexpr int imin(int a, int b) { return a < b ? a : b; }
constexpr int imax(int a, int b) { return a > b ? a : b; }
constexpr int cnt1(int p) { return imin(8 * NTILE, imax(8 * B2 + (p + 1) * RAMP8 / 8, p + 1 + UF)); }
constexpr bool sched1_ok() {
  for (int p = -1; p < 8 * T_RING; ++p) {
    const int t = (cnt1(p) - 1) >> 3;
    if (t >= T_RING + RS) return false;
    if (t >= NR && t < T_RING && p <= 8 * (RS + t - NR) + 3) return false;
  }
  return cnt1(8 * T_RING - 1) == 8 * T_RING + UF;
}
static_assert(sched1_ok(), "pass-1 schedule");
constexpr int cnt2(int p) { return p < -13 ? 0 : (p < -9 ? imin(UF, 4) : imin(8 * NSTREAM, imax(UF, p + 1 + UF))); }
constexpr bool parked2(int u) { return u >= PARK_AT && (u - PARK_AT) % PARK_STEP == 0 && (u - PARK_AT) / PARK_STEP < NPARK; }
constexpr int tile2(int u) {
  int np = 0;
  for (int i = 0; i < u; ++i) np += parked2(i) ? 1 : 0;
  return parked2(u) ? NTILE - RS - 1 - np : T_RING + u - np;
}
constexpr int T_TAIL = (NTILE - RS - NPARK - 1) / RS * RS;
static_assert(NPARK == 0 || PARK_AT + PARK_STEP * (NPARK - 1) <= NKEEP - RS, "parked tiles sit in the unrolled part of pass 2");
constexpr int slot1(int t) { return t < NR ? t : (t < T_RING ? RS + (t - NR) : (t - T_RING) % RS); }

#define POLL_HOOK(slot)
#define POLL_DONE(slot)
#define POLL_BEGIN { bool ok_ = false; for (unsigned spins_ = 0;; ++spins_) { asm volatile("" ::: "memory"); if (!ok_) {
#define POLL_END(ok, slot) ok_ = (ok); } POLL_HOOK(slot) if (__builtin_amdgcn_ballot_w64(!ok_) == 0ull || spins_ > SPIN_CAP) { POLL_DONE(slot) break; } __builtin_amdgcn_s_sleep(1); } }

constexpr int AUX_SC1 = 16;

#define STAMP(i) do { if ((i) == 0 || (i) == 1 || (i) == 3 || (i) == 11 || (i) == 13) tt[(i) == 0 ? 0 : (i) == 1 ? 1 : (i) == 3 ? 2 : (i) == 11 ? 3 : 4] = (unsigned)__builtin_amdgcn_s_memrealtime(); } while (0)

__global__ void __launch_bounds__(256, 1)
fused_decode_kernel(const unsigned short* __restrict__ xin, const unsigned char* __restrict__ kc, const unsigned char* __restrict__ vc,
                    const unsigned char* __restrict__ wq, const unsigned char* __restrict__ wo, const unsigned char* __restrict__ wg,
                    const unsigned char* __restrict__ wu, const unsigned char* __restrict__ wd, unsigned short* __restrict__ out,
                    unsigned char* ws) {
  __shared__ __attribute__((aligned(16))) unsigned char smem[L_BYTES];
  const int b = blockIdx.x;
  unsigned tt[5];
  const int w = __builtin_amdgcn_readfirstlane(threadIdx.x >> 6);
  const int head = b >> 4, split = b & 15;
  const int myr = b - (NWG - NRED);
  const unsigned lbase = (unsigned)(size_t)smem;

  const v4i rsK = mk_rsrc(kc + ((size_t)head << 24), 1u << 24);
  const v4i rsV = mk_rsrc(vc + ((size_t)head << 24), 1u << 24);
  const v4i rsQ = mk_rsrc(wq, DM * NQ * 2);
  const v4i rsC = mk_rsrc(ws, WS_BYTES);
  const __amdgpu_buffer_rsrc_t rsW = __builtin_amdgcn_make_buffer_rsrc(ws, 0, WS_BYTES, 0x00020000);

  const unsigned m0base = lbase + L_KS + w * (NL * 4096);
  const unsigned soffw = (split << 20) + w * 4096;
  const unsigned rot = (unsigned)(ROT_EXPR) & 63u;
  auto toff = [&](unsigned t) { return soffw + (((t + rot) & 63u) << 14); };
  int lane, g, c, tid;
  Masks mk;
  unsigned voffV, voffK, addrK;
  auto relane = [&]() {
    const Ln L = make_ln();
    lane = L.lane; g = L.g; c = L.c; mk = L.mk; tid = w * 64 + lane;
    voffV = lane * 16;
    voffK = g * 256 + ((c ^ g) << 4);
    const unsigned kap = 4 * (c & 3) + (c >> 2);
    addrK = m0base + kap * 256 + (((4 * g) ^ kap) << 4);
  };
  relane();
  STAMP(0);

  Tile KR[NR], VR[NR], VL[NL];

  unsigned due = 0;
  int lag = 0;
  auto now8 = [&]() { return (unsigned)__builtin_amdgcn_s_memrealtime() << 8; };
  auto gate = [&](unsigned pace = PACE) {
    if constexpr (PACE != 0) {
      due += (PACE_MASK + 1) * pace;
      while ((lag = (int)(now8() - due)) < 0) __builtin_amdgcn_s_sleep(1);
    }
  };
  if constexpr (PACE != 0) due = now8() + PACE_OFF1 * 256;
  auto issue1 = [&](auto N) {
    constexpr int n = N.value, t = n >> 3, i = n & 7;
    if constexpr (n >= 8 && (n & PACE_MASK) == 0) gate(t <= T_RING ? PACE_EARLY : PACE1);
    const unsigned so = toff(t);
    constexpr bool park = t >= NTILE - RS - NPARK && t < NTILE - RS;
    if constexpr (i < 4) issue_k<i, park>(m0base + slot1(t) * 4096, voffK, rsK, so);
    else if constexpr (t < NR) issue_v<i - 4, vr_in_a(t) ? 0 : 1>(VR[t].r[i - 4], voffV, rsV, so);
    else if constexpr (t < T_RING + RS) issue_v<i - 4, 0>(VL[slot1(t)].r[i - 4], voffV, rsV, so);
    else issue_v<i - 4, park ? 3 : 2>(VL[slot1(t)].r[i - 4], voffV, rsV, so);
  };
  auto issue2 = [&](auto N) {
    constexpr int n = N.value, u = n >> 3, i = n & 7;
    if constexpr (n >= 8 && (n & PACE_MASK) == 0) gate(parked2(u) ? PARK_PACE : PACE);
    const unsigned so = toff(tile2(u));
    if constexpr (i < 4) issue_k<i>(m0base + (u % RS) * 4096, voffK, rsK, so);
    else issue_v<i - 4, 2>(VL[u % RS].r[i - 4], voffV, rsV, so);
  };

  const int qs = QSMAP ? 64 * ((b & 7) >> 1) + 4 * (b >> 4) + 2 * (b & 1) + ((b >> 3) & 1) : (b & 7) * 32 + (b >> 3);
  v2u wqf[12];
  auto issue_wq = [&]() {
    const unsigned voffQ = (192u * w + (4 * g + (c >> 2)) * 12u) * (NQ * 2) + (unsigned)(qs * 16 + 4 * (c & 3)) * 2u;
    sfor<0, 12>([&](auto I) {
      load_b64(wqf[I.value], voffQ, rsQ, I.value * NQ * 2);
    });
  };

  auto layernorm = [&](v2u (&px)[3]) {
    const bool act2 = lane < 16;
    auto up = [](unsigned pr) { return (f2){bf_lo(pr), bf_hi(pr)}; };
    f2 s = (up(px[0][0]) + up(px[0][1])) + (up(px[1][0]) + up(px[1][1])) + (up(px[2][0]) + up(px[2][1]));
    const float mean = wave_sum(s[0] + s[1]) * (1.f / DM);
    opaque3(px);
    f2 q = {0.f, 0.f}, q2 = {0.f, 0.f};
#pragma unroll
    for (int k = 0; k < 3; ++k) {
#pragma unroll
      for (int i = 0; i < 2; ++i) {
        const f2 a = up(px[k][i]) - mean;
        if (k < 2) q = __builtin_elementwise_fma(a, a, q); else q2 = __builtin_elementwise_fma(a, a, q2);
      }
    }
    const float var = wave_sum((q[0] + q[1]) + (act2 ? q2[0] + q2[1] : 0.f)) * (1.f / DM);
    opaque3(px);
    const float mb = rbf(mean);
    const float den = rbf(__builtin_amdgcn_sqrtf(rbf(rbf(var) + 1e-5f)));
    const float rc = __builtin_amdgcn_rcpf(den);
    auto dv = [&](unsigned pr) {
      const unsigned a2 = pk_bf(bf_lo(pr) - mb, bf_hi(pr) - mb);
      const f2 a = {bf_lo(a2), bf_hi(a2)};
      const f2 q0 = a * rc;
      const f2 q1 = __builtin_elementwise_fma(__builtin_elementwise_fma(-q0, (f2){den, den}, a), (f2){rc, rc}, q0);
      return pk_bf(q1[0], q1[1]);
    };
#pragma unroll
    for (int k = 0; k < 3; ++k) {
      v2u o;
      o[0] = dv(px[k][0]);
      o[1] = dv(px[k][1]);
      if (k < 2 || act2) *(v2u*)(smem + L_XB + (lane + 64 * k) * 8) = o;
      __builtin_amdgcn_sched_barrier(0);
    }
  };

  auto q_gemv = [&]() {
    if (w < 3) {
      const v2u* xp = (const v2u*)(smem + L_XB + (192 * w + (4 * g + (c >> 2)) * 12) * 2);
      const v2u xa = xp[0], xb = xp[1], xc = xp[2];
      const unsigned xs[6] = {xa[0], xa[1], xb[0], xb[1], xc[0], xc[1]};
      const unsigned ma = mk.m0 | mk.m2, mb = mk.m1 | mk.m3;
      f4 d;
#pragma unroll
      for (int n = 0; n < 6; ++n) {
        const unsigned lo = dup_lo(xs[n]), hi = dup_hi(xs[n]);
        const v4u bb = {lo & ma, lo & mb, hi & ma, hi & mb};
        const v4u aa = {wqf[2 * n][0], wqf[2 * n][1], wqf[2 * n + 1][0], wqf[2 * n + 1][1]};
        if (n == 0) d = mfma_first(aa, bb); else mfma_acc(d, aa, bb);
      }
      mfma_fence(d);
      if ((c >> 2) == g) {
        float* rp = (float*)(smem + L_REDA) + (w * 4 + g) * 16 + (c & 3);
#pragma unroll
        for (int r = 0; r < 4; ++r) rp[4 * r] = d[r];
      }
    }
  };
  auto q_exchange = [&](unsigned tag) {
    constexpr bool dl_ = false;
    if (lane < 8) {
      const f2* rp = (const f2*)(smem + L_REDA) + lane;
      f2 a = rp[0];
#pragma unroll
      for (int i = 1; i < 12; ++i) a += rp[8 * i];
      v2u gr = {pk_bf(a[0], a[1]), tag};
      __builtin_amdgcn_raw_buffer_store_b64(gr, rsW, WS_QG + (qs * 8 + lane) * 8, 0, AUX_SC1);
    }
    v4u v = {0u, 0u, 0u, 0u};
    if constexpr (DELAY_Q > 0) __builtin_amdgcn_s_sleep(DELAY_Q);
    POLL_BEGIN
      ldc1(v, rsC, WS_QG + head * 1024 + lane * 16);
    POLL_END(tagged(v, tag), -1)
    v2u o = {v[0], v[2]};
    *(v2u*)(smem + L_QB + lane * 8) = o;
  };

  unsigned tagbase;
  auto TAG = [&](unsigned ph) { return 0x80000000u | ((tagbase + ph) & 0x7fffffffu); };
  auto lds_wait = [&](int word) { while (lds_get(lbase + L_FLAG + 4 * word) == 0u) __builtin_amdgcn_s_sleep(1); };
  auto paced1 = [&](auto N) { wait_issue<UFPRE - 1>(lag); issue1(N); };
  {
    v2u px[3];
    unsigned ep;
    const v4i rsX = mk_rsrc(xin, DM * 2);
    const v4i rsE = mk_rsrc(ws, 64);
    asm volatile("buffer_load_dwordx2 %0, %4, %5, 0 offen\n\t"
                 "buffer_load_dwordx2 %1, %4, %5, 0 offen offset:512\n\t"
                 "buffer_load_dwordx2 %2, %4, %5, 0 offen offset:1024\n\t"
                 "buffer_load_dword %3, off, %6, 0 sc1"
                 : "=&v"(px[0]), "=&v"(px[1]), "=&v"(px[2]), "=&v"(ep)
                 : "v"(lane * 8), "s"(rsX), "s"(rsE) : "memory");
    issue_wq();
    sfor<0, 8>(issue1);
    if constexpr (CODE_WARM) {
      unsigned long long pc;
      unsigned len;
      asm volatile("s_getpc_b64 %0\n.Lfd_code_from:\n\ts_mov_b32 %1, .Lfd_code_to-.Lfd_code_from" : "=s"(pc), "=s"(len));
      const v4i rsP = mk_rsrc((const void*)pc, len);
      if (w == 2) touch(lbase + L_REDB, lane * 64, rsP, (b >> 3) * 4096);
    }
    if (tid < 8) *(unsigned*)(smem + L_FLAG + 4 * tid) = 0u;
    wg_barrier();
    if (w == 3) {
      asm volatile("s_waitcnt vmcnt(%3)" : "+v"(px[0]), "+v"(px[1]), "+v"(px[2]) : "i"(20) : "memory");
#pragma unroll
      for (int k = 0; k < 3; ++k) if (((lane + 64 * k) >> 2) == myr) *(v2u*)(smem + L_XR + (lane & 3) * 8) = px[k];
      layernorm(px);
      lds_put(lbase + L_FLAG, 1u);
    }
    sfor<8, P1>(paced1);
    wait_wq<UFPRE>(wqf);
    asm volatile("" : "+v"(ep));
    tagbase = __builtin_amdgcn_readfirstlane(ep);
  }
  if (w < 3) {
    lds_wait(0);
    q_gemv();
    lds_put(lbase + L_FLAG + 4 + 4 * w, 1u);
    STAMP(23);
  } else {
    lds_wait(1); lds_wait(2); lds_wait(3);
    q_exchange(TAG(1));
    lds_put(lbase + L_FLAG + 16, 1u);
    STAMP(24);
  }
  sfor<P1, 8 * B2>(paced1);
  lds_wait(4);
  STAMP(1);
  {
    const v4u z = {0u, 0u, 0u, 0u};
    if (tid < 144) __builtin_amdgcn_raw_buffer_store_b128(z, rsW, WS_PA + (head * 16 + tid / 9) * PA_STRIDE + split * PA_MSG + (tid % 9) * 16, 0, AUX_SC1);
    __builtin_amdgcn_raw_buffer_store_b128(z, rsW, WS_PB + (tid >> 3) * PB_STRIDE + b * 128 + (tid & 7) * 16, 0, AUX_SC1);
    if (tid < 32) __builtin_amdgcn_raw_buffer_store_b128(z, rsW, WS_PB + (32 + (tid >> 3)) * PB_STRIDE + b * 128 + (tid & 7) * 16, 0, AUX_SC1);
    if (myr >= 0 && tid < 4 * XREP) __builtin_amdgcn_raw_buffer_store_b128(z, rsW, WS_XG + (tid >> 2) * XG_STRIDE + myr * 64 + (tid & 3) * 16, 0, AUX_SC1);
  }
  Tile Q;
  Soft st;
  auto load_q = [&]() {
    const v4u* qp = (const v4u*)(smem + L_QB + (c >> 3) * 256 + g * 64);
#pragma unroll
    for (int j = 0; j < 4; ++j) Q.r[j] = qp[j];
    st.acc = (f4){0.f, 0.f, 0.f, 0.f};
    st.mc = -__builtin_inff();
    st.l = 0.f;
  };

  auto ring_step = [&](auto S, unsigned t, unsigned pace) {
    constexpr int s = S.value;
    const unsigned so0 = toff(t), so1 = toff(t + 1);
    f4 p;
    sfor<0, 8>([&](auto I) {
      constexpr int i = I.value, dt = (UF + i) >> 3, ii = (UF + i) & 7, ns = (s + dt) % RS;
      wait_issue<UF - 1>(lag);
      if constexpr ((ii & PACE_MASK) == 0) gate(pace);
      if constexpr (ii < 4) issue_k<ii>(m0base + ns * 4096, voffK, rsK, dt ? so1 : so0);
      else issue_v<ii - 4, 2>(VL[ns].r[ii - 4], voffV, rsV, dt ? so1 : so0);
      if constexpr (i == 3) { Tile kt; read_k<s>(kt, addrK); p = softmax_tile(st, qk(kt, Q)); }
      if constexpr (i == 7) pv_tile<true>(st, p, VL[s], mk);
    });
  };
  auto step1 = [&](auto T) {
    constexpr int t = T.value;
    f4 p;
    sfor<0, 8>([&](auto I) {
      constexpr int i = I.value, pt = 8 * t + i;
      if constexpr (cnt1(pt) > cnt1(pt - 1)) wait_issue<cnt1(pt - 1) - (pt + 1)>(lag);
      else if constexpr (i == 3 || i == 7) wait_vm<cnt1(pt - 1) - (pt + 1) + (t == NTILE - 1 ? ((WO_EARLY & 1) ? 6 : 0) + 3 * WARM1 : 0)>();
      sfor<cnt1(pt - 1), cnt1(pt)>(issue1);
      if constexpr (i == 3) {
        if constexpr (t < NR) { read_k<slot1(t)>(KR[t], addrK); p = softmax_tile(st, qk(KR[t], Q)); }
        else { Tile kt; read_k<slot1(t)>(kt, addrK); p = softmax_tile(st, qk(kt, Q)); }
      }
      if constexpr (i == 7) {
        if constexpr (t < NR) pv_tile<vr_in_a(t)>(st, p, VR[t], mk); else pv_tile<true>(st, p, VL[slot1(t)], mk);
      }
    });
  };
  auto step2 = [&](auto U) {
    constexpr int u = U.value, s = (u + RS) % RS;
    f4 p;
    sfor<0, 8>([&](auto I) {
      constexpr int i = I.value, pt = 8 * u + i;
      if constexpr (pt >= 0 && cnt2(pt) > cnt2(pt - 1)) wait_issue<cnt2(pt - 1) - (pt + 1)>(lag);
      else if constexpr (pt >= 0 && (i == 3 || i == 7)) wait_vm<cnt2(pt - 1) - (pt + 1) + (u == NSTREAM - 1 && (WO_EARLY & 2) ? 6 : 0)>();
      if constexpr (pt >= 0 || (i != 3 && i != 7)) sfor<cnt2(pt - 1), cnt2(pt)>(issue2);
      if constexpr (i == 3) { Tile kt; read_k<s>(kt, addrK); if constexpr (pt < 0) sfor<cnt2(pt - 1), cnt2(pt)>(issue2); p = softmax_tile(st, qk(kt, Q)); }
      if constexpr (i == 7) { pv_tile<true>(st, p, VL[s], mk); if constexpr (pt < 0) sfor<cnt2(pt - 1), cnt2(pt)>(issue2); }
    });
  };
  using I0 = std::integral_constant<int, 0>;
  using I1 = std::integral_constant<int, 1>;

  auto warm = [&](int wi) {
    const v4i rsG = mk_rsrc(wg, DM * DFF * 2), rsU = mk_rsrc(wu, DM * DFF * 2), rsD = mk_rsrc(wd, DM * DFF * 2);
    const unsigned q = (b >> 3) * 1536u + tid + 256 * wi, r = q / 72u;
    const unsigned vg = r * (DFF * 2) + (q - r * 72u) * 16u;
    touch16(lbase + L_PAD, vg, rsG, (b & 7) * 1152u);
    touch16(lbase + L_PAD, vg, rsU, (b & 7) * 1152u);
    touch16(lbase + L_PAD, q < 41472u ? q * 16u : OOB, rsD, (b & 7) * (576u * DM * 2));
  };
  v4u wof[4], woc[2];
  auto load_wo = [&]() {
    const unsigned char* p = wo + (size_t)(b * 16 + 4 * g) * (DM * 2);
#pragma unroll
    for (int n = 0; n < 4; ++n) wof[n] = ld_w<(WDEF & 4) != 0>(p + n * (DM * 2) + 256 * w + 16 * c);
#pragma unroll
    for (int n = 0; n < 2; ++n) woc[n] = ld_w<(WDEF & 4) != 0>(p + (2 * n + (c >> 3)) * (DM * 2) + 1024 + 16 * (c & 7));
    asm volatile("" ::: "memory");
  };
  auto chain = [&](auto LAYER) {
    constexpr int layer = LAYER.value;
    constexpr bool dl_ = layer == DLAYER;
    relane();
    STAMP(3 + 10 * layer);
    const unsigned ph = 2 + 5 * layer;

    const int mu = !MUMAP ? (b & 7) * 18 + (b >> 3) : (b < 128 ? 16 * (b & 7) + (b >> 3) : (b < 136 ? 128 + 2 * (b & 7) : 129 + 2 * ((b + 4) & 7)));
    constexpr int GW = layer == 0 ? GATE_WIN : 27;
    v4u wb[GW];
    const unsigned char* pgu;
    unsigned odn, odc;
    auto bases = [&]() {
      pgu = (((c >> 2) & 1) ? wu : wg) + (size_t)(144 * w + (2 * g + (c >> 3)) * 18) * (DFF * 2) + 64 * mu + 16 * (c & 3);
      odn = (32 * mu + 8 * w + 2 * g) * (DM * 2) + 16 * c;
      odc = (32 * mu + 8 * w + 2 * g + (c >> 3)) * (DM * 2) + 1024 + 16 * (c & 7);
    };
    auto frag = [&](auto F) {
      constexpr int f = F.value;
      if constexpr (f < 18) wb[f % GW] = ld_w<(WDEF & 1) != 0>(pgu + (size_t)f * (DFF * 2));
      else if constexpr (f < 26) wb[f % GW] = ld_w<(WDEF & 2) != 0>(wd + odn + ((f - 18) & 1) * (DM * 2) + ((f - 18) >> 1) * 256);
      else if constexpr (f == 26) wb[f % GW] = ld_w<(WDEF & 2) != 0>(wd + odc);
    };
    constexpr int EF = layer == 0 ? EARLY_F0 : EARLY_F1;
    if (b < NMLP) { bases(); sfor<0, EF>(frag); }
    {
      const float lt = xrow_sum(st.l);
      float* cp = (float*)(smem + L_REDB) + w * 264;
#pragma unroll
      for (int r = 0; r < 4; ++r) cp[(c >> 3) * 128 + 32 * g + 8 * r + (c & 7)] = st.acc[r];
      if ((lane & 0x37) == 0) { f2 ml = {st.mc, lt}; *(f2*)(cp + 256 + 2 * (c >> 3)) = ml; }
    }
    wg_barrier();
    {
      const float* cp = (const float*)(smem + L_REDB);
      const int h = tid >> 7;
      f2 ml[4];
#pragma unroll
      for (int i = 0; i < 4; ++i) ml[i] = *(const f2*)(cp + i * 264 + 256 + 2 * h);
      const float M = fmaxf(fmaxf(ml[0][0], ml[1][0]), fmaxf(ml[2][0], ml[3][0]));
      float O = 0.f, L = 0.f;
#pragma unroll
      for (int i = 0; i < 4; ++i) { const float sc = ex2(ml[i][0] - M); O += sc * cp[i * 264 + tid]; L += sc * ml[i][1]; }
      const unsigned tg = TAG(ph);
      const unsigned pa = WS_PA + (head * 16 + (tid >> 4)) * PA_STRIDE + split * PA_MSG;
      v2u gr = {__float_as_uint(O), tg};
      __builtin_amdgcn_raw_buffer_store_b64(gr, rsW, pa + c * 8, 0, AUX_SC1);
      if (c == 0) {
        v4u g2 = {__float_as_uint(M), tg, __float_as_uint(L), tg};
        __builtin_amdgcn_raw_buffer_store_b128(g2, rsW, pa + 128, 0, AUX_SC1);
      }
    }
    STAMP(4 + 10 * layer);
    relane();
    {
      const unsigned tg = TAG(ph);
      const unsigned base = WS_PA + b * PA_STRIDE + c * PA_MSG;
      v4u a[3];
#pragma unroll
      for (int k = 0; k < 3; ++k) a[k] = (v4u){0u, 0u, 0u, 0u};
      if constexpr (DELAY_PA > 0) __builtin_amdgcn_s_sleep(DELAY_PA);
      POLL_BEGIN
        ldc3<16>(a, rsC, base + 32 * g, base + 128);
      POLL_END(tagged(a[0], tg) && tagged(a[1], tg) && tagged(a[2], tg), 26)
      STAMP(5 + 10 * layer);
      const float ms = __uint_as_float(a[2][0]);
      const float wgt = ex2(ms - row_max(ms));
      const float inv = frcp(row_sum(wgt * __uint_as_float(a[2][2])));
      float o[4];
      o[0] = row_sum(wgt * __uint_as_float(a[0][0])) * inv;
      o[1] = row_sum(wgt * __uint_as_float(a[0][2])) * inv;
      o[2] = row_sum(wgt * __uint_as_float(a[1][0])) * inv;
      o[3] = row_sum(wgt * __uint_as_float(a[1][2])) * inv;
      f4 d = mfma_first(wof[0], diag(pk_bf(o[0], o[0]), mk));
#pragma unroll
      for (int n = 1; n < 4; ++n) mfma_acc(d, wof[n], diag(pk_bf(o[n], o[n]), mk));
      mfma_fence(d);
      const unsigned tb = TAG(ph + 1);
      const bool hp = (c >> 3) != 0;
      auto put_line = [&](float lo, float hi, unsigned red, bool act) {
        const float lon = dpp<0xB1>(lo), hin = dpp<0xB1>(hi);
        const bool odd = (c & 1) != 0;
        v4u gr = {__float_as_uint(odd ? hin : lo), tb, __float_as_uint(odd ? hi : lon), tb};
        __builtin_amdgcn_raw_buffer_store_b128(gr, rsW, act ? WS_PB + red * PB_STRIDE + b * 128 + (odd ? 64 : 0) + (c & 6) * 8 : OOB, 0, AUX_SC1);
      };
      put_line(hp ? d[2] : d[0], hp ? d[3] : d[1], 8 * w + 2 * g + (hp ? 1 : 0), true);
      if (w == 0) {
        const float oa = hp ? o[1] : o[0], obv = hp ? o[3] : o[2];
        f4 e = mfma_first(woc[0], diag(pk_bf(oa, oa), mk));
        mfma_acc(e, woc[1], diag(pk_bf(obv, obv), mk));
        mfma_fence(e);
        float f[4];
#pragma unroll
        for (int r = 0; r < 4; ++r) {
          const float own = (g >> 1) == (hp ? 1 : 0) ? e[r] : 0.f;
          const float t = own + dpp<0x128>(own);
          auto sw = __builtin_amdgcn_permlane32_swap(__float_as_uint(t), __float_as_uint(t), false, false);
          f[r] = __uint_as_float(sw[0]) + __uint_as_float(sw[1]);
        }
        put_line(hp ? f[2] : f[0], hp ? f[3] : f[1], 32 + 2 * g + (hp ? 1 : 0), g < 2);
      }
    }
    STAMP(6 + 10 * layer);
    relane();
    if constexpr (layer == 0 && WARM2 > WARM1) {
#pragma unroll
      for (int i = WARM1; i < WARM2; ++i) warm(i);
      wait_vm<WARM_WAIT>();
    }
    v2u xres;
    auto reduce_cols = [&](auto NPc, unsigned tg) {
      constexpr int NP = NPc.value, NK = (NP * 8 + 255) / 256;
      v4u a[NK];
#pragma unroll
      for (int k = 0; k < NK; ++k) a[k] = (v4u){0u, 0u, 0u, 0u};
      static_assert(NK == 8 || NK == 5, "two shapes");
      const unsigned base = WS_PB + myr * PB_STRIDE;
      const bool off5 = tid + 1024 >= NP * 8;
      bool m;
      if constexpr (NK == 8 && DELAY_PB > 0) __builtin_amdgcn_s_sleep(DELAY_PB);
      POLL_BEGIN
        ldc4(a[0], a[1], a[2], a[3], rsC, tid * 16, base);
        if constexpr (NK == 8) { ldc4(a[4], a[5], a[6], a[7], rsC, tid * 16, base + 16384); pin4(a[4], a[5], a[6], a[7]); }
        else ldc1(a[4], rsC, off5 ? OOB : base + 16384 + tid * 16);
        pin4(a[0], a[1], a[2], a[3]);
        m = true;
#pragma unroll
        for (int k = 0; k < NK; ++k) m = m && (tagged(a[k], tg) || (NK == 5 && k == 4 && off5));
      POLL_END(m, NP == NWG ? 28 : 30)
      f2 s = {0.f, 0.f};
#pragma unroll
      for (int k = 0; k < NK; ++k) { s[0] += __uint_as_float(a[k][0]); s[1] += __uint_as_float(a[k][2]); }
#pragma unroll
      for (int i = 0; i < 2; ++i) { s[i] += dpp<0x128>(s[i]); s[i] = xrow_sum(s[i]); }
      if (lane < 8) ((f2*)(smem + L_REDA))[w * 8 + lane] = s;
      wg_barrier();
      const f4* rp = (const f4*)(smem + L_REDA) + (tid & 3);
      const f4 t = (rp[0] + rp[4]) + (rp[8] + rp[12]);
      if (w == 0) {
        const v2u xo = *(const v2u*)(smem + L_XR + (tid & 3) * 8);
        xres[0] = pk_bf(bf_lo(xo[0]) + rbf(t[0]), bf_hi(xo[0]) + rbf(t[1]));
        xres[1] = pk_bf(bf_lo(xo[1]) + rbf(t[2]), bf_hi(xo[1]) + rbf(t[3]));
        if (tid < 4) *(v2u*)(smem + L_XR + tid * 8) = xres;
      }
    };
    auto publish_x = [&](unsigned tg) {
      if (tid < 4 * XREP) {
        v4u gx = {xres[0], tg, xres[1], tg};
        __builtin_amdgcn_raw_buffer_store_b128(gx, rsW, WS_XG + (tid >> 2) * XG_STRIDE + myr * 64 + (tid & 3) * 16, 0, AUX_SC1);
      }
    };
    auto gather_x_ln = [&](unsigned tg) {
      v4u a[3];
#pragma unroll
      for (int k = 0; k < 3; ++k) a[k] = (v4u){0u, 0u, 0u, 0u};
      const unsigned xb = WS_XG + (b % XREP) * XG_STRIDE;
      POLL_BEGIN
        ldc3<1024>(a, rsC, xb + lane * 16, lane < 16 ? xb + 2048 + lane * 16 : OOB);
      POLL_END(tagged(a[0], tg) && tagged(a[1], tg) && (tagged(a[2], tg) || lane >= 16), tg == TAG(ph + 2) ? 28 : -1)
      v2u px[3];
#pragma unroll
      for (int k = 0; k < 3; ++k) { px[k][0] = a[k][0]; px[k][1] = a[k][2]; }
      layernorm(px);
    };

    if (b < NMLP) {
      bases();
      sfor<EF, GW>(frag);
      if (w == 3) gather_x_ln(TAG(ph + 2));
      relane();
      bases();
      wg_barrier();
      STAMP(8 + 10 * layer);
      f4 d;
      {
        const unsigned* xp = (const unsigned*)(smem + L_XB + (144 * w + (2 * g + (c >> 3)) * 18) * 2);
        unsigned xs[9];
        constexpr int XA = layer == 0 ? 4 : 9;
#pragma unroll
        for (int n = 0; n < XA; ++n) xs[n] = xp[n];
        sfor<0, 18>([&](auto Nn) {
          constexpr int n = Nn.value;
          const v4u bb = diag((n & 1) ? dup_hi(xs[n >> 1]) : dup_lo(xs[n >> 1]), mk);
          if constexpr ((n & 1) && (n >> 1) + XA < 9) xs[(n >> 1) + XA] = xp[(n >> 1) + XA];
          if constexpr (n == 0) d = mfma_first(wb[0], bb); else mfma_acc(d, wb[n % GW], bb);
          frag(std::integral_constant<int, n + GW>{});
          if constexpr (layer == 0) __builtin_amdgcn_sched_barrier(0);
        });
      }
      mfma_fence(d);
      if ((g >> 1) == (c >> 3)) {
        float* rp = (float*)(smem + L_REDA) + (w * 4 + g) * 32 + (c & 7);
#pragma unroll
        for (int r = 0; r < 4; ++r) rp[8 * r] = d[r];
      }
      wg_barrier();
      relane();
      unsigned hd0, hd1;
      const bool hp = (c >> 3) != 0;
      {
        f2 v = ((const f2*)(smem + L_REDA))[4 * w + g + 32 * (c & 7) + 16 * (c >> 3)];
        float hv[2];
#pragma unroll
        for (int i = 0; i < 2; ++i) {
          float t = v[i];
          t += dpp<0xB1>(t); t += dpp<0x4E>(t); t += dpp<0x141>(t);
          const float y = dpp<0x128>(t);
          const float x = rbf(hp ? y : t), up = rbf(hp ? t : y);
          const float z = 0.7978845608028654f * (x + 0.044715f * x * x * x);
          const float th = 1.f - 2.f * __builtin_amdgcn_rcpf(__expf(2.f * z) + 1.f);
          hv[i] = rbf(0.5f * x * (1.f + th)) * up;
        }
        hd0 = pk_bf(hv[0], hv[0]); hd1 = pk_bf(hv[1], hv[1]);
      }
      {
        const v4u b0 = diag(hd0, mk), b1 = diag(hd1, mk);
        float* rp = (float*)(smem + L_REDB);
        f4 e[5];
#pragma unroll
        for (int bl = 0; bl < 4; ++bl) {
          e[bl] = mfma_first(wb[(18 + 2 * bl) % GW], b0);
          mfma_acc(e[bl], wb[(19 + 2 * bl) % GW], b1);
        }
        e[4] = mfma_first(wb[26 % GW], diag(hp ? hd1 : hd0, mk));
        mfma_fence5(e);
#pragma unroll
        for (int bl = 0; bl < 4; ++bl) {
          float* q = rp + w * 512 + 128 * bl + 32 * g + (hp ? 16 : 0) + (c & 7);
          q[0] = hp ? e[bl][2] : e[bl][0]; q[8] = hp ? e[bl][3] : e[bl][1];
        }
        if ((g >> 1) == (c >> 3)) {
          float* q = rp + 2048 + w * 128 + 32 * g + (c & 7);
#pragma unroll
          for (int r = 0; r < 4; ++r) q[8 * r] = e[4][r];
        }
      }
      wg_barrier();
      {
        const f2* rp = (const f2*)(smem + L_REDB);
        const unsigned tg = TAG(ph + 3);
        const f2 a = (rp[tid] + rp[256 + tid]) + (rp[512 + tid] + rp[768 + tid]);
        v4u g0 = {__float_as_uint(a[0]), tg, __float_as_uint(a[1]), tg};
        __builtin_amdgcn_raw_buffer_store_b128(g0, rsW, WS_PB + (tid >> 3) * PB_STRIDE + mu * 128 + (tid & 7) * 16, 0, AUX_SC1);
        if (tid < 32) {
          f2 e = rp[1024 + tid];
#pragma unroll
          for (int i = 1; i < 8; ++i) e += rp[1024 + 32 * i + tid];
          v4u g1 = {__float_as_uint(e[0]), tg, __float_as_uint(e[1]), tg};
          __builtin_amdgcn_raw_buffer_store_b128(g1, rsW, WS_PB + (32 + (tid >> 3)) * PB_STRIDE + mu * 128 + (tid & 7) * 16, 0, AUX_SC1);
        }
      }
      STAMP(9 + 10 * layer);
      if constexpr (layer == 0) issue_wq();
    } else {
      if (myr >= 0) {
        reduce_cols(std::integral_constant<int, NWG>{}, TAG(ph + 1));
        publish_x(TAG(ph + 2));
        STAMP(7 + 10 * layer);
      }
      if constexpr (layer == 0) issue_wq();
      if (myr >= 0) {
        reduce_cols(std::integral_constant<int, NMLP>{}, TAG(ph + 3));
        if constexpr (layer == 0) publish_x(TAG(ph + 4));
        else if (tid < 4) {
          *(v2u*)(out + 16 * myr + 4 * tid) = xres;
          if (myr == 0 && tid == 0) __builtin_amdgcn_raw_buffer_store_b32(tagbase + 16u, rsW, WS_EPOCH, 0, AUX_SC1);
        }
        STAMP(10 + 10 * layer);
      }
    }
    if constexpr (layer == 0) {
      relane();
      if (w == 3) gather_x_ln(TAG(ph + 4));
      wg_barrier();
      STAMP(25);
      wait_wq<0>(wqf);
      STAMP(21);
      q_gemv();
      STAMP(22);
      wg_barrier();
      if (w == 3) q_exchange(TAG(ph + 5));
      wg_barrier();
      STAMP(11);
    }
  };

  load_q();
  sfor<0, T_RING>(step1);
  STAMP(2);
  sfor<0, 4 * RS>([&](auto J) { if constexpr (8 * (J.value >> 2) + 4 + (J.value & 3) >= UF) def_a(VL[J.value >> 2].r[J.value & 3]); });
#pragma unroll 1
  for (int t = T_RING; t < T_TAIL; t += RS) sfor<0, RS>([&](auto S) { ring_step(S, t + S.value, PACE1); });
  sfor<T_TAIL, NTILE - 1>(step1);
  if constexpr (WO_EARLY & 1) load_wo();
#pragma unroll
  for (int i = 0; i < WARM1; ++i) warm(i);
  step1(std::integral_constant<int, NTILE - 1>{});
  if constexpr (!(WO_EARLY & 1)) load_wo();
  chain(I0{});
  relane();
  if constexpr (PACE != 0) due = now8() + PACE_OFF2 * 256;
  load_q();
  static_assert(NKEEP % RS == 0 && NSTREAM % RS == 0, "ring parity");
  sfor<0, NKEEP>([&](auto J) {
    constexpr int i = J.value;
    step2(std::integral_constant<int, i - RS>{});
    if constexpr (i < NR) {
      const f4 p = softmax_tile(st, qk(KR[i], Q));
      pv_tile<vr_in_a(i)>(st, p, VR[i], mk);
    } else {
      Tile kt;
      read_k<RS + (i - NR)>(kt, addrK);
      const f4 p = softmax_tile(st, qk(kt, Q));
      pv_tile<true>(st, p, VL[RS + (i - NR)], mk);
    }
  });
  STAMP(12);
#pragma unroll 1
  for (int u = NKEEP - RS; u < NSTREAM - RS; u += RS) sfor<0, RS>([&](auto S) { ring_step(S, T_RING + u + S.value - NPARK, PACE); });
  step2(std::integral_constant<int, NSTREAM - 2>{});
  if constexpr (WO_EARLY & 2) load_wo();
  step2(std::integral_constant<int, NSTREAM - 1>{});
  if constexpr (!(WO_EARLY & 2)) load_wo();
  chain(I1{});
  asm volatile(".Lfd_code_to:");
  { unsigned te = (unsigned)__builtin_amdgcn_s_memrealtime(); if ((b % 37 == 0 || b == 255) && threadIdx.x == 0) printf("b %d entry %u q1 +%u p1 +%u q2 +%u p2 +%u end +%u\n", b, tt[0] % 100000u, tt[1] - tt[0], tt[2] - tt[0], tt[3] - tt[0], tt[4] - tt[0], te - tt[0]); }
}

}

extern "C" void kernel_launch(void* const* d_in, const int* in_sizes, int n_in, void* d_out, int out_size, void* d_ws, size_t ws_size,
                              hipStream_t stream) {
  (void)in_sizes; (void)n_in; (void)out_size; (void)ws_size;
  fused_decode_kernel<<<NWG, 256, 0, stream>>>(
      (const unsigned short*)d_in[0], (const unsigned char*)d_in[1], (const unsigned char*)d_in[2], (const unsigned char*)d_in[3],
      (const unsigned char*)d_in[4], (const unsigned char*)d_in[6], (const unsigned char*)d_in[7], (const unsigned char*)d_in[8],
      (unsigned short*)d_out, (unsigned char*)d_ws);
}
```

```cpp
#define FLOOR_X 0
#define FLOOR_Y 0
#define FLOOR_Z 0
#include <hip/hip_runtime.h>
#include <hip/hip_bf16.h>
#include <type_traits>

#define DIAG 0

#define QSMAP 0
#define MUMAP 0
#define EARLY_F0 0
#define EARLY_F1 0
#define WO_EARLY 3
#define DELAY_PA 6
#define DELAY_PB 12
#define DELAY_Q 6
#define CODE_WARM 1
#define PARK_AT 2
#define PARK_STEP 3
#define PARK_PACE 1600
#define WARM1 3
#define WARM2 6
#define WARM_WAIT 0
#define GRADED 1
#define LAG1 30
#define LAG2 150
#define LAG3 400
#define NPARK 0
#define WDEF 7
namespace {

typedef unsigned v4u __attribute__((ext_vector_type(4)));
typedef unsigned v2u __attribute__((ext_vector_type(2)));
typedef int v4i __attribute__((ext_vector_type(4)));
typedef float f4 __attribute__((ext_vector_type(4)));
typedef float f2 __attribute__((ext_vector_type(2)));
typedef __bf16 bf2 __attribute__((ext_vector_type(2)));
#define GAS __attribute__((address_space(1)))
#define FI __device__ __forceinline__

constexpr int DM = 576, DFF = 4608, NQ = 4096;
constexpr int NWG = 256;
constexpr int NMLP = 144;
constexpr int NRED = 36;
constexpr int XREP = 8;
constexpr int NTILE = 64;
constexpr int NR = 9;
constexpr int NL = 9;
#define DLAYER 0
#define GATE_WIN 12
#define INFLIGHT 8
constexpr int UF = INFLIGHT;
constexpr int RS = 2;
constexpr int NSTREAM = NTILE - NR - NL;
#define BURST2 6
#define UFPRE 8
#define RAMP8 3
#define PRE1 20
constexpr int P1 = PRE1;
constexpr int B2 = BURST2;
constexpr int T_RING = NR + NL - RS;
static_assert(GATE_WIN >= 9, "all down fragments must fit the window");
static_assert(PRE1 >= 8 + UFPRE, "the wq wait counts on UFPRE younger loads");
static_assert(UF == 8 || WO_EARLY == 0, "the last tile has been requested in full before the wo loads go out");
static_assert(UF >= 2 && UF <= 8 && B2 <= NR, "schedule");
#define PACE 3552
#define ROT_EXPR 0
#define PACE1 3584
#define PACE_EARLY 3712
#define PACE_OFF1 200
#define PACE_OFF2 30
#define PACE_MASK 3
constexpr int NKEEP = NR + NL - RS;
constexpr float QSCALE = 0.08838834764831845f * 1.4426950408889634f;
constexpr unsigned SPIN_CAP = 200000u;

constexpr unsigned WS_EPOCH = 0;
constexpr unsigned WS_QG = 4096;
constexpr unsigned WS_XG = 32768, XG_STRIDE = 4096;
constexpr unsigned WS_PA = 65536 + 32768, PA_MSG = 160, PA_STRIDE = 16 * PA_MSG;
constexpr unsigned WS_PB = 1u << 20, PB_STRIDE = NWG * 128;
constexpr unsigned WS_BYTES = WS_PB + NRED * PB_STRIDE;
constexpr unsigned OOB = 0x7fffff00u;
constexpr unsigned WS_DIAG = 4u << 20;

constexpr int L_KS = 0;
constexpr int L_SC = 4 * NL * 4096;
constexpr int L_REDB = L_SC;
constexpr int L_REDA = L_SC + 10240;
constexpr int L_XB = L_REDA + 2048;
constexpr int L_QB = L_XB + 1280;
constexpr int L_PAD = L_QB + 512;
constexpr int L_FLAG = L_PAD + 1024;
constexpr int L_XR = L_FLAG + 32;
constexpr int L_BYTES = L_XR + 32;
static_assert(L_BYTES <= 163840, "LDS");

template <int B, int E, class F> FI void sfor(F&& f) {
  if constexpr (B < E) { f(std::integral_constant<int, B>{}); sfor<B + 1, E>(f); }
}

FI v4i mk_rsrc(const void* p, unsigned bytes) {
  unsigned long long a = (unsigned long long)p;
  v4i r = {(int)(unsigned)a, (int)((unsigned)(a >> 32) & 0xffffu), (int)bytes, 0x00020000};
  return r;
}
FI float bf_lo(unsigned u) { return __uint_as_float(u << 16); }
FI float bf_hi(unsigned u) { return __uint_as_float(u & 0xffff0000u); }
FI unsigned pk_bf(float a, float b) { f2 v = {a, b}; return __builtin_bit_cast(unsigned, __builtin_convertvector(v, bf2)); }
FI float rbf(float a) { return bf_lo(pk_bf(a, a)); }
FI float frcp(float x) { const float r = __builtin_amdgcn_rcpf(x); return __builtin_fmaf(__builtin_fmaf(-x, r, 1.f), r, r); }
FI float ex2(float x) { return __builtin_amdgcn_exp2f(x); }

template <int CTRL> FI float dpp(float x) {
  return __builtin_bit_cast(float, __builtin_amdgcn_mov_dpp(__builtin_bit_cast(int, x), CTRL, 0xf, 0xf, true));
}
FI float row_sum(float x) {
  x += dpp<0xB1>(x); x += dpp<0x4E>(x); x += dpp<0x141>(x); x += dpp<0x128>(x); return x;
}
FI float row_max(float x) {
  x = fmaxf(x, dpp<0xB1>(x)); x = fmaxf(x, dpp<0x4E>(x)); x = fmaxf(x, dpp<0x141>(x)); x = fmaxf(x, dpp<0x128>(x)); return x;
}
FI float xrow_max(float x) {
  auto s = __builtin_amdgcn_permlane16_swap(__float_as_uint(x), __float_as_uint(x), false, false);
  x = fmaxf(__uint_as_float(s[0]), __uint_as_float(s[1]));
  auto t = __builtin_amdgcn_permlane32_swap(__float_as_uint(x), __float_as_uint(x), false, false);
  return fmaxf(__uint_as_float(t[0]), __uint_as_float(t[1]));
}
FI float xrow_sum(float x) {
  auto s = __builtin_amdgcn_permlane16_swap(__float_as_uint(x), __float_as_uint(x), false, false);
  x = __uint_as_float(s[0]) + __uint_as_float(s[1]);
  auto t = __builtin_amdgcn_permlane32_swap(__float_as_uint(x), __float_as_uint(x), false, false);
  return __uint_as_float(t[0]) + __uint_as_float(t[1]);
}
FI float wave_sum(float x) { return xrow_sum(row_sum(x)); }

FI void wg_barrier() { asm volatile("s_waitcnt lgkmcnt(0)\n\ts_barrier" ::: "memory"); }
template <int N> FI void wait_vm() { asm volatile("s_waitcnt vmcnt(%0)" ::"i"(N > 63 ? 63 : N) : "memory"); }

template <int N> FI void wait_issue(int lag) {
  if constexpr (!GRADED || N != 7) wait_vm<N>();
  else
    asm volatile(
        "s_cmp_gt_i32 %0, %3\n\ts_cbranch_scc1 1f\n\ts_waitcnt vmcnt(6)\n\t"
        "s_cmp_gt_i32 %0, %2\n\ts_cbranch_scc1 1f\n\ts_waitcnt vmcnt(5)\n\t"
        "s_cmp_gt_i32 %0, %1\n\ts_cbranch_scc1 1f\n\ts_waitcnt vmcnt(4)\n"
        "1:\n\ts_waitcnt vmcnt(7)"
        ::"s"(lag), "i"(LAG1 * 256), "i"(LAG2 * 256), "i"(LAG3 * 256) : "memory", "scc");
}

FI void mfma_acc(f4& d, const v4u& a, const v4u& b) {
  asm("s_nop 1\n\tv_mfma_f32_16x16x32_bf16 %0, %1, %2, %0" : "+v"(d) : "v"(a), "v"(b));
}
FI f4 mfma_first(const v4u& a, const v4u& b) {
  f4 d;
  asm("s_nop 1\n\tv_mfma_f32_16x16x32_bf16 %0, %1, %2, 0" : "=&v"(d) : "v"(a), "v"(b));
  return d;
}
FI void mfma_fence(f4& d) { asm("s_nop 7\n\ts_nop 1" : "+v"(d)); }

FI void mfma_fence5(f4 (&e)[5]) { asm("s_nop 7\n\ts_nop 1" : "+v"(e[0]), "+v"(e[1]), "+v"(e[2]), "+v"(e[3]), "+v"(e[4])); }

struct Masks { unsigned m0, m1, m2, m3; };
FI v4u diag(unsigned dup, const Masks& k) { v4u b = {dup & k.m0, dup & k.m1, dup & k.m2, dup & k.m3}; return b; }
FI unsigned dup_lo(unsigned x) { return __builtin_amdgcn_perm(x, x, 0x01000100u); }
FI unsigned dup_hi(unsigned x) { return __builtin_amdgcn_perm(x, x, 0x03020302u); }

struct Tile { v4u r[4]; };

struct Ln {
  int lane, g, c;
  Masks mk;
};
FI Ln make_ln() {
  Ln L;
  asm volatile("v_mbcnt_lo_u32_b32 %0, -1, 0\n\tv_mbcnt_hi_u32_b32 %0, -1, %0" : "=v"(L.lane));
  L.g = L.lane >> 4; L.c = L.lane & 15;
  const unsigned hm = (L.c & 1) ? 0xffff0000u : 0x0000ffffu;
  const int e2 = (L.c & 7) >> 1;
  L.mk.m0 = e2 == 0 ? hm : 0u; L.mk.m1 = e2 == 1 ? hm : 0u; L.mk.m2 = e2 == 2 ? hm : 0u; L.mk.m3 = e2 == 3 ? hm : 0u;
  return L;
}

#define K_LOAD(POL) \
  "s_mov_b32 %0, m0\n\t" \
  "s_mov_b32 m0, %2\n\t" \
  "v_xor_b32 %1, %6, %3\n\t" \
  "buffer_load_dwordx4 %1, %4, %5 offen offset:%7 " POL " lds\n\t" \
  "s_mov_b32 m0, %0"
template <int I, bool KEEP = false> FI void issue_k(unsigned m0v, unsigned voff, const v4i& rs, unsigned soff) {
  unsigned keep, tmp;
  if constexpr (KEEP)
    asm volatile(K_LOAD("") : "=&s"(keep), "=&v"(tmp) : "s"(m0v), "v"(voff), "s"(rs), "s"(soff), "i"(64 * I), "i"(1024 * I) : "memory");
  else
    asm volatile(K_LOAD("nt") : "=&s"(keep), "=&v"(tmp) : "s"(m0v), "v"(voff), "s"(rs), "s"(soff), "i"(64 * I), "i"(1024 * I) : "memory");
}
template <int I, int MODE> FI void issue_v(v4u& t, unsigned voff, const v4i& rs, unsigned soff) {
  if constexpr (MODE == 0)
    asm volatile("buffer_load_dwordx4 %0, %1, %2, %3 offen offset:%4 nt" : "=a"(t) : "v"(voff), "s"(rs), "s"(soff), "i"(1024 * I) : "memory");
  else if constexpr (MODE == 1)
    asm volatile("buffer_load_dwordx4 %0, %1, %2, %3 offen offset:%4 nt" : "=&v"(t) : "v"(voff), "s"(rs), "s"(soff), "i"(1024 * I) : "memory");
  else if constexpr (MODE == 2)
    asm volatile("buffer_load_dwordx4 %0, %1, %2, %3 offen offset:%4 nt" ::"a"(t), "v"(voff), "s"(rs), "s"(soff), "i"(1024 * I) : "memory");
  else
    asm volatile("buffer_load_dwordx4 %0, %1, %2, %3 offen offset:%4" ::"a"(t), "v"(voff), "s"(rs), "s"(soff), "i"(1024 * I) : "memory");
}
FI void def_a(v4u& t) { asm volatile("" : "=a"(t)); }
FI void touch(unsigned m0v, unsigned voff, const v4i& rs, unsigned soff) {
  unsigned keep;
  asm volatile(
      "s_mov_b32 %0, m0\n\t"
      "s_mov_b32 m0, %1\n\t"
      "s_nop 0\n\t"
      "buffer_load_dword %2, %3, %4 offen lds\n\t"
      "s_mov_b32 m0, %0"
      : "=&s"(keep)
      : "s"(m0v), "v"(voff), "s"(rs), "s"(soff)
      : "memory");
}
FI void touch16(unsigned m0v, unsigned voff, const v4i& rs, unsigned soff) {
  unsigned keep;
  asm volatile(
      "s_mov_b32 %0, m0\n\t"
      "s_mov_b32 m0, %1\n\t"
      "s_nop 0\n\t"
      "buffer_load_dwordx4 %2, %3, %4 offen lds\n\t"
      "s_mov_b32 m0, %0"
      : "=&s"(keep)
      : "s"(m0v), "v"(voff), "s"(rs), "s"(soff)
      : "memory");
}
FI void opaque3(v2u (&p)[3]) { asm volatile("" : "+v"(p[0]), "+v"(p[1]), "+v"(p[2])); }
FI void ldc1(v4u& a, const v4i& rs, unsigned v0) {
  asm volatile("buffer_load_dwordx4 %0, %2, %1, 0 offen sc1\n\ts_waitcnt vmcnt(0)" : "+v"(a) : "s"(rs), "v"(v0) : "memory");
}
template <int IMM> FI void ldc3(v4u (&a)[3], const v4i& rs, unsigned v0, unsigned v2) {
  asm volatile(
      "buffer_load_dwordx4 %0, %4, %3, 0 offen sc1\n\t"
      "buffer_load_dwordx4 %1, %4, %3, 0 offen offset:%6 sc1\n\t"
      "buffer_load_dwordx4 %2, %5, %3, 0 offen sc1\n\t"
      "s_waitcnt vmcnt(0)"
      : "+v"(a[0]), "+v"(a[1]), "+v"(a[2]) : "s"(rs), "v"(v0), "v"(v2), "i"(IMM) : "memory");
}
FI void ldc4(v4u& a0, v4u& a1, v4u& a2, v4u& a3, const v4i& rs, unsigned v0, unsigned so) {
  asm volatile(
      "buffer_load_dwordx4 %0, %5, %4, %6 offen sc1\n\t"
      "buffer_load_dwordx4 %1, %5, %4, %7 offen sc1\n\t"
      "buffer_load_dwordx4 %2, %5, %4, %8 offen sc1\n\t"
      "buffer_load_dwordx4 %3, %5, %4, %9 offen sc1"
      : "+v"(a0), "+v"(a1), "+v"(a2), "+v"(a3) : "s"(rs), "v"(v0), "s"(so), "s"(so + 4096u), "s"(so + 8192u), "s"(so + 12288u) : "memory");
}
FI void pin4(v4u& a0, v4u& a1, v4u& a2, v4u& a3) { asm volatile("s_waitcnt vmcnt(0)" : "+v"(a0), "+v"(a1), "+v"(a2), "+v"(a3) :: "memory"); }
FI bool tagged(const v4u& a, unsigned tg) { return a[1] == tg && a[3] == tg; }
FI void lds_put(unsigned addr, unsigned v) { asm volatile("s_waitcnt lgkmcnt(0)\n\tds_write_b32 %0, %1" ::"v"(addr), "v"(v) : "memory"); }
FI unsigned lds_get(unsigned addr) {
  unsigned v;
  asm volatile("ds_read_b32 %0, %1\n\ts_waitcnt lgkmcnt(0)" : "=v"(v) : "v"(addr) : "memory");
  return __builtin_amdgcn_readfirstlane(v);
}
template <bool DEF> FI v4u ld_w(const unsigned char* p) { if constexpr (DEF) return *(const v4u*)p; else return __builtin_nontemporal_load((const v4u*)p); }
template <int SLOT> FI void read_k(Tile& k, unsigned addr) {
  unsigned tmp;
  asm volatile(
      "ds_read_b128 %0, %5 offset:%6\n\t"
      "v_xor_b32 %4, 16, %5\n\t"
      "ds_read_b128 %1, %4 offset:%6\n\t"
      "v_xor_b32 %4, 32, %5\n\t"
      "ds_read_b128 %2, %4 offset:%6\n\t"
      "v_xor_b32 %4, 48, %5\n\t"
      "ds_read_b128 %3, %4 offset:%6\n\t"
      "s_waitcnt lgkmcnt(0)"
      : "=&v"(k.r[0]), "=&v"(k.r[1]), "=&v"(k.r[2]), "=&v"(k.r[3]), "=&v"(tmp)
      : "v"(addr), "i"(SLOT * 4096)
      : "memory");
}
FI f4 qk(const Tile& k, const Tile& q) {
  f4 s;
  asm volatile(
      "v_mfma_f32_16x16x32_bf16 %0, %1, %5, 0\n\t"
      "v_mfma_f32_16x16x32_bf16 %0, %2, %6, %0\n\t"
      "v_mfma_f32_16x16x32_bf16 %0, %3, %7, %0\n\t"
      "v_mfma_f32_16x16x32_bf16 %0, %4, %8, %0\n\t"
      "s_nop 7\n\ts_nop 1"
      : "=&v"(s)
      : "v"(k.r[0]), "v"(k.r[1]), "v"(k.r[2]), "v"(k.r[3]), "v"(q.r[0]), "v"(q.r[1]), "v"(q.r[2]), "v"(q.r[3]));
  return s;
}
FI void load_b64(v2u& d, unsigned voff, const v4i& rs, unsigned soff) {
  asm volatile("buffer_load_dwordx2 %0, %1, %2, %3 offen nt" : "=v"(d) : "v"(voff), "s"(rs), "s"(soff) : "memory");
}
template <int N> FI void wait_wq(v2u (&q)[12]) {
  asm volatile("s_waitcnt vmcnt(%12)"
               : "+v"(q[0]), "+v"(q[1]), "+v"(q[2]), "+v"(q[3]), "+v"(q[4]), "+v"(q[5]), "+v"(q[6]), "+v"(q[7]), "+v"(q[8]), "+v"(q[9]),
                 "+v"(q[10]), "+v"(q[11])
               : "i"(N) : "memory");
}

struct Soft { f4 acc; float mc, l; };

FI f4 softmax_tile(Soft& st, const f4& s) {
  f4 t;
#pragma unroll
  for (int r = 0; r < 4; ++r) t[r] = __builtin_fmaf(s[r], QSCALE, -st.mc);
  const float tm = fmaxf(fmaxf(t[0], t[1]), fmaxf(t[2], t[3]));
  if (__builtin_amdgcn_ballot_w64(tm > 0.f) != 0ull) {
    const float sm = fmaxf(fmaxf(s[0], s[1]), fmaxf(s[2], s[3])) * QSCALE;
    const float mn = fmaxf(st.mc, xrow_max(sm));
    const float al = ex2(st.mc - mn);
    st.mc = mn;
    st.l *= al;
#pragma unroll
    for (int r = 0; r < 4; ++r) { st.acc[r] *= al; t[r] = __builtin_fmaf(s[r], QSCALE, -mn); }
  }
  f4 p;
#pragma unroll
  for (int r = 0; r < 4; ++r) p[r] = ex2(t[r]);
  st.l += (p[0] + p[1]) + (p[2] + p[3]);
  return p;
}
#define PV_MFMAS \
  "s_nop 1\n\t" \
  "v_mfma_f32_16x16x32_bf16 %0, %1, %5, %0\n\t" \
  "v_mfma_f32_16x16x32_bf16 %0, %2, %6, %0\n\t" \
  "v_mfma_f32_16x16x32_bf16 %0, %3, %7, %0\n\t" \
  "v_mfma_f32_16x16x32_bf16 %0, %4, %8, %0\n\t" \
  "s_nop 7\n\ts_nop 1"
template <bool A> FI void pv_tile(Soft& st, const f4& p, const Tile& v, const Masks& k) {
  const v4u b0 = diag(pk_bf(p[0], p[0]), k), b1 = diag(pk_bf(p[1], p[1]), k), b2 = diag(pk_bf(p[2], p[2]), k), b3 = diag(pk_bf(p[3], p[3]), k);
  if constexpr (A)
    asm volatile(PV_MFMAS : "+v"(st.acc) : "a"(v.r[0]), "a"(v.r[1]), "a"(v.r[2]), "a"(v.r[3]), "v"(b0), "v"(b1), "v"(b2), "v"(b3));
  else
    asm volatile(PV_MFMAS : "+v"(st.acc) : "v"(v.r[0]), "v"(v.r[1]), "v"(v.r[2]), "v"(v.r[3]), "v"(b0), "v"(b1), "v"(b2), "v"(b3));
}

constexpr bool vr_in_a(int i) { return i < 7; }

constexpr int imin(int a, int b) { return a < b ? a : b; }
constexpr int imax(int a, int b) { return a > b ? a : b; }
constexpr int cnt1(int p) { return imin(8 * NTILE, imax(8 * B2 + (p + 1) * RAMP8 / 8, p + 1 + UF)); }
constexpr bool sched1_ok() {
  for (int p = -1; p < 8 * T_RING; ++p) {
    const int t = (cnt1(p) - 1) >> 3;
    if (t >= T_RING + RS) return false;
    if (t >= NR && t < T_RING && p <= 8 * (RS + t - NR) + 3) return false;
  }
  return cnt1(8 * T_RING - 1) == 8 * T_RING + UF;
}
static_assert(sched1_ok(), "pass-1 schedule");
constexpr int cnt2(int p) { return p < -13 ? 0 : (p < -9 ? imin(UF, 4) : imin(8 * NSTREAM, imax(UF, p + 1 + UF))); }
constexpr bool parked2(int u) { return u >= PARK_AT && (u - PARK_AT) % PARK_STEP == 0 && (u - PARK_AT) / PARK_STEP < NPARK; }
constexpr int tile2(int u) {
  int np = 0;
  for (int i = 0; i < u; ++i) np += parked2(i) ? 1 : 0;
  return parked2(u) ? NTILE - RS - 1 - np : T_RING + u - np;
}
constexpr int T_TAIL = (NTILE - RS - NPARK - 1) / RS * RS;
static_assert(NPARK == 0 || PARK_AT + PARK_STEP * (NPARK - 1) <= NKEEP - RS, "parked tiles sit in the unrolled part of pass 2");
constexpr int slot1(int t) { return t < NR ? t : (t < T_RING ? RS + (t - NR) : (t - T_RING) % RS); }

#define POLL_HOOK(slot)
#define POLL_DONE(slot)
#define POLL_BEGIN { bool ok_ = false; for (unsigned spins_ = 0;; ++spins_) { asm volatile("" ::: "memory"); if (!ok_) {
#define POLL_END(ok, slot) ok_ = (ok); } POLL_HOOK(slot) if (__builtin_amdgcn_ballot_w64(!ok_) == 0ull || spins_ > SPIN_CAP) { POLL_DONE(slot) break; } __builtin_amdgcn_s_sleep(1); } }

constexpr int AUX_SC1 = 16;

#define STAMP(i) do {} while (0)

__global__ void __launch_bounds__(256, 1)
fused_decode_kernel(const unsigned short* __restrict__ xin, const unsigned char* __restrict__ kc, const unsigned char* __restrict__ vc,
                    const unsigned char* __restrict__ wq, const unsigned char* __restrict__ wo, const unsigned char* __restrict__ wg,
                    const unsigned char* __restrict__ wu, const unsigned char* __restrict__ wd, unsigned short* __restrict__ out,
                    unsigned char* ws) {
  __shared__ __attribute__((aligned(16))) unsigned char smem[L_BYTES];
  const int b = blockIdx.x;
  const int w = __builtin_amdgcn_readfirstlane(threadIdx.x >> 6);
  const int head = b >> 4, split = b & 15;
  const int myr = b - (NWG - NRED);
  const unsigned lbase = (unsigned)(size_t)smem;

  const v4i rsK = mk_rsrc(kc + ((size_t)head << 24), 1u << 24);
  const v4i rsV = mk_rsrc(vc + ((size_t)head << 24), 1u << 24);
  const v4i rsQ = mk_rsrc(wq, DM * NQ * 2);
  const v4i rsC = mk_rsrc(ws, WS_BYTES);
  const __amdgpu_buffer_rsrc_t rsW = __builtin_amdgcn_make_buffer_rsrc(ws, 0, WS_BYTES, 0x00020000);

  const unsigned m0base = lbase + L_KS + w * (NL * 4096);
  const unsigned soffw = (split << 20) + w * 4096;
  const unsigned rot = (unsigned)(ROT_EXPR) & 63u;
  auto toff = [&](unsigned t) { return soffw + (((t + rot) & 63u) << 14); };
  int lane, g, c, tid;
  Masks mk;
  unsigned voffV, voffK, addrK;
  auto relane = [&]() {
    const Ln L = make_ln();
    lane = L.lane; g = L.g; c = L.c; mk = L.mk; tid = w * 64 + lane;
    voffV = lane * 16;
    voffK = g * 256 + ((c ^ g) << 4);
    const unsigned kap = 4 * (c & 3) + (c >> 2);
    addrK = m0base + kap * 256 + (((4 * g) ^ kap) << 4);
  };
  relane();
  STAMP(0);

  Tile KR[NR], VR[NR], VL[NL];

  unsigned due = 0;
  int lag = 0;
  auto now8 = [&]() { return (unsigned)__builtin_amdgcn_s_memrealtime() << 8; };
  auto gate = [&](unsigned pace = PACE) {
    if constexpr (PACE != 0) {
      due += (PACE_MASK + 1) * pace;
      while ((lag = (int)(now8() - due)) < 0) __builtin_amdgcn_s_sleep(1);
    }
  };
  const unsigned t_in = (unsigned)__builtin_amdgcn_s_memrealtime();
  if constexpr (PACE != 0) due = now8() + PACE_OFF1 * 256;
  auto issue1 = [&](auto N) {
    constexpr int n = N.value, t = n >> 3, i = n & 7;
    if constexpr (n >= 8 && (n & PACE_MASK) == 0) gate(t <= T_RING ? PACE_EARLY : PACE1);
    const unsigned so = toff(t);
    constexpr bool park = t >= NTILE - RS - NPARK && t < NTILE - RS;
    if constexpr (i < 4) issue_k<i, park>(m0base + slot1(t) * 4096, voffK, rsK, so);
    else if constexpr (t < NR) issue_v<i - 4, vr_in_a(t) ? 0 : 1>(VR[t].r[i - 4], voffV, rsV, so);
    else if constexpr (t < T_RING + RS) issue_v<i - 4, 0>(VL[slot1(t)].r[i - 4], voffV, rsV, so);
    else issue_v<i - 4, park ? 3 : 2>(VL[slot1(t)].r[i - 4], voffV, rsV, so);
  };
  auto issue2 = [&](auto N) {
    constexpr int n = N.value, u = n >> 3, i = n & 7;
    if constexpr (n >= 8 && (n & PACE_MASK) == 0) gate(parked2(u) ? PARK_PACE : PACE);
    const unsigned so = toff(tile2(u));
    if constexpr (i < 4) issue_k<i>(m0base + (u % RS) * 4096, voffK, rsK, so);
    else issue_v<i - 4, 2>(VL[u % RS].r[i - 4], voffV, rsV, so);
  };

  const int qs = QSMAP ? 64 * ((b & 7) >> 1) + 4 * (b >> 4) + 2 * (b & 1) + ((b >> 3) & 1) : (b & 7) * 32 + (b >> 3);
  v2u wqf[12];
  auto issue_wq = [&]() {
    const unsigned voffQ = (192u * w + (4 * g + (c >> 2)) * 12u) * (NQ * 2) + (unsigned)(qs * 16 + 4 * (c & 3)) * 2u;
    sfor<0, 12>([&](auto I) {
      load_b64(wqf[I.value], voffQ, rsQ, I.value * NQ * 2);
    });
  };

  auto layernorm = [&](v2u (&px)[3]) {
    const bool act2 = lane < 16;
    auto up = [](unsigned pr) { return (f2){bf_lo(pr), bf_hi(pr)}; };
    f2 s = (up(px[0][0]) + up(px[0][1])) + (up(px[1][0]) + up(px[1][1])) + (up(px[2][0]) + up(px[2][1]));
    const float mean = wave_sum(s[0] + s[1]) * (1.f / DM);
    opaque3(px);
    f2 q = {0.f, 0.f}, q2 = {0.f, 0.f};
#pragma unroll
    for (int k = 0; k < 3; ++k) {
#pragma unroll
      for (int i = 0; i < 2; ++i) {
        const f2 a = up(px[k][i]) - mean;
        if (k < 2) q = __builtin_elementwise_fma(a, a, q); else q2 = __builtin_elementwise_fma(a, a, q2);
      }
    }
    const float var = wave_sum((q[0] + q[1]) + (act2 ? q2[0] + q2[1] : 0.f)) * (1.f / DM);
    opaque3(px);
    const float mb = rbf(mean);
    const float den = rbf(__builtin_amdgcn_sqrtf(rbf(rbf(var) + 1e-5f)));
    const float rc = __builtin_amdgcn_rcpf(den);
    auto dv = [&](unsigned pr) {
      const unsigned a2 = pk_bf(bf_lo(pr) - mb, bf_hi(pr) - mb);
      const f2 a = {bf_lo(a2), bf_hi(a2)};
      const f2 q0 = a * rc;
      const f2 q1 = __builtin_elementwise_fma(__builtin_elementwise_fma(-q0, (f2){den, den}, a), (f2){rc, rc}, q0);
      return pk_bf(q1[0], q1[1]);
    };
#pragma unroll
    for (int k = 0; k < 3; ++k) {
      v2u o;
      o[0] = dv(px[k][0]);
      o[1] = dv(px[k][1]);
      if (k < 2 || act2) *(v2u*)(smem + L_XB + (lane + 64 * k) * 8) = o;
      __builtin_amdgcn_sched_barrier(0);
    }
  };

  auto q_gemv = [&]() {
    if (w < 3) {
      const v2u* xp = (const v2u*)(smem + L_XB + (192 * w + (4 * g + (c >> 2)) * 12) * 2);
      const v2u xa = xp[0], xb = xp[1], xc = xp[2];
      const unsigned xs[6] = {xa[0], xa[1], xb[0], xb[1], xc[0], xc[1]};
      const unsigned ma = mk.m0 | mk.m2, mb = mk.m1 | mk.m3;
      f4 d;
#pragma unroll
      for (int n = 0; n < 6; ++n) {
        const unsigned lo = dup_lo(xs[n]), hi = dup_hi(xs[n]);
        const v4u bb = {lo & ma, lo & mb, hi & ma, hi & mb};
        const v4u aa = {wqf[2 * n][0], wqf[2 * n][1], wqf[2 * n + 1][0], wqf[2 * n + 1][1]};
        if (n == 0) d = mfma_first(aa, bb); else mfma_acc(d, aa, bb);
      }
      mfma_fence(d);
      if ((c >> 2) == g) {
        float* rp = (float*)(smem + L_REDA) + (w * 4 + g) * 16 + (c & 3);
#pragma unroll
        for (int r = 0; r < 4; ++r) rp[4 * r] = d[r];
      }
    }
  };
  auto q_exchange = [&](unsigned tag) {
    constexpr bool dl_ = false;
    if (lane < 8) {
      const f2* rp = (const f2*)(smem + L_REDA) + lane;
      f2 a = rp[0];
#pragma unroll
      for (int i = 1; i < 12; ++i) a += rp[8 * i];
      v2u gr = {pk_bf(a[0], a[1]), tag};
      __builtin_amdgcn_raw_buffer_store_b64(gr, rsW, WS_QG + (qs * 8 + lane) * 8, 0, AUX_SC1);
    }
    v4u v = {0u, 0u, 0u, 0u};
    if constexpr (DELAY_Q > 0) __builtin_amdgcn_s_sleep(DELAY_Q);
    POLL_BEGIN
      ldc1(v, rsC, WS_QG + head * 1024 + lane * 16);
    POLL_END(tagged(v, tag), -1)
    v2u o = {v[0], v[2]};
    *(v2u*)(smem + L_QB + lane * 8) = o;
  };

  unsigned tagbase;
  auto TAG = [&](unsigned ph) { return 0x80000000u | ((tagbase + ph) & 0x7fffffffu); };
  auto lds_wait = [&](int word) { while (lds_get(lbase + L_FLAG + 4 * word) == 0u) __builtin_amdgcn_s_sleep(1); };
  auto paced1 = [&](auto N) { wait_issue<UFPRE - 1>(lag); issue1(N); };
  {
    v2u px[3];
    unsigned ep;
    const v4i rsX = mk_rsrc(xin, DM * 2);
    const v4i rsE = mk_rsrc(ws, 64);
    asm volatile("buffer_load_dwordx2 %0, %4, %5, 0 offen\n\t"
                 "buffer_load_dwordx2 %1, %4, %5, 0 offen offset:512\n\t"
                 "buffer_load_dwordx2 %2, %4, %5, 0 offen offset:1024\n\t"
                 "buffer_load_dword %3, off, %6, 0 sc1"
                 : "=&v"(px[0]), "=&v"(px[1]), "=&v"(px[2]), "=&v"(ep)
                 : "v"(lane * 8), "s"(rsX), "s"(rsE) : "memory");
    issue_wq();
    sfor<0, 8>(issue1);
    if constexpr (CODE_WARM) {
      unsigned long long pc;
      unsigned len;
      asm volatile("s_getpc_b64 %0\n.Lfd_code_from:\n\ts_mov_b32 %1, .Lfd_code_to-.Lfd_code_from" : "=s"(pc), "=s"(len));
      const v4i rsP = mk_rsrc((const void*)pc, len);
      if (w == 2) touch(lbase + L_REDB, lane * 64, rsP, (b >> 3) * 4096);
    }
    if (tid < 8) *(unsigned*)(smem + L_FLAG + 4 * tid) = 0u;
    wg_barrier();
    if (w == 3) {
      asm volatile("s_waitcnt vmcnt(%3)" : "+v"(px[0]), "+v"(px[1]), "+v"(px[2]) : "i"(20) : "memory");
#pragma unroll
      for (int k = 0; k < 3; ++k) if (((lane + 64 * k) >> 2) == myr) *(v2u*)(smem + L_XR + (lane & 3) * 8) = px[k];
      layernorm(px);
      lds_put(lbase + L_FLAG, 1u);
    }
    sfor<8, P1>(paced1);
    wait_wq<UFPRE>(wqf);
    asm volatile("" : "+v"(ep));
    tagbase = __builtin_amdgcn_readfirstlane(ep);
  }
  if (w < 3) {
    lds_wait(0);
    q_gemv();
    lds_put(lbase + L_FLAG + 4 + 4 * w, 1u);
    STAMP(23);
  } else {
    lds_wait(1); lds_wait(2); lds_wait(3);
    q_exchange(TAG(1));
    lds_put(lbase + L_FLAG + 16, 1u);
    STAMP(24);
  }
  sfor<P1, 8 * B2>(paced1);
  lds_wait(4);
  STAMP(1);
  {
    const v4u z = {0u, 0u, 0u, 0u};
    if (tid < 144) __builtin_amdgcn_raw_buffer_store_b128(z, rsW, WS_PA + (head * 16 + tid / 9) * PA_STRIDE + split * PA_MSG + (tid % 9) * 16, 0, AUX_SC1);
    __builtin_amdgcn_raw_buffer_store_b128(z, rsW, WS_PB + (tid >> 3) * PB_STRIDE + b * 128 + (tid & 7) * 16, 0, AUX_SC1);
    if (tid < 32) __builtin_amdgcn_raw_buffer_store_b128(z, rsW, WS_PB + (32 + (tid >> 3)) * PB_STRIDE + b * 128 + (tid & 7) * 16, 0, AUX_SC1);
    if (myr >= 0 && tid < 4 * XREP) __builtin_amdgcn_raw_buffer_store_b128(z, rsW, WS_XG + (tid >> 2) * XG_STRIDE + myr * 64 + (tid & 3) * 16, 0, AUX_SC1);
  }
  Tile Q;
  Soft st;
  auto load_q = [&]() {
    const v4u* qp = (const v4u*)(smem + L_QB + (c >> 3) * 256 + g * 64);
#pragma unroll
    for (int j = 0; j < 4; ++j) Q.r[j] = qp[j];
    st.acc = (f4){0.f, 0.f, 0.f, 0.f};
    st.mc = -__builtin_inff();
    st.l = 0.f;
  };

  auto ring_step = [&](auto S, unsigned t, unsigned pace) {
    constexpr int s = S.value;
    const unsigned so0 = toff(t), so1 = toff(t + 1);
    f4 p;
    sfor<0, 8>([&](auto I) {
      constexpr int i = I.value, dt = (UF + i) >> 3, ii = (UF + i) & 7, ns = (s + dt) % RS;
      wait_issue<UF - 1>(lag);
      if constexpr ((ii & PACE_MASK) == 0) gate(pace);
      if constexpr (ii < 4) issue_k<ii>(m0base + ns * 4096, voffK, rsK, dt ? so1 : so0);
      else issue_v<ii - 4, 2>(VL[ns].r[ii - 4], voffV, rsV, dt ? so1 : so0);
      if constexpr (i == 3) { Tile kt; read_k<s>(kt, addrK); p = softmax_tile(st, qk(kt, Q)); }
      if constexpr (i == 7) pv_tile<true>(st, p, VL[s], mk);
    });
  };
  auto step1 = [&](auto T) {
    constexpr int t = T.value;
    f4 p;
    sfor<0, 8>([&](auto I) {
      constexpr int i = I.value, pt = 8 * t + i;
      if constexpr (cnt1(pt) > cnt1(pt - 1)) wait_issue<cnt1(pt - 1) - (pt + 1)>(lag);
      else if constexpr (i == 3 || i == 7) wait_vm<cnt1(pt - 1) - (pt + 1) + (t == NTILE - 1 ? ((WO_EARLY & 1) ? 6 : 0) + 3 * WARM1 : 0)>();
      sfor<cnt1(pt - 1), cnt1(pt)>(issue1);
      if constexpr (i == 3) {
        if constexpr (t < NR) { read_k<slot1(t)>(KR[t], addrK); p = softmax_tile(st, qk(KR[t], Q)); }
        else { Tile kt; read_k<slot1(t)>(kt, addrK); p = softmax_tile(st, qk(kt, Q)); }
      }
      if constexpr (i == 7) {
        if constexpr (t < NR) pv_tile<vr_in_a(t)>(st, p, VR[t], mk); else pv_tile<true>(st, p, VL[slot1(t)], mk);
      }
    });
  };
  auto step2 = [&](auto U) {
    constexpr int u = U.value, s = (u + RS) % RS;
    f4 p;
    sfor<0, 8>([&](auto I) {
      constexpr int i = I.value, pt = 8 * u + i;
      if constexpr (pt >= 0 && cnt2(pt) > cnt2(pt - 1)) wait_issue<cnt2(pt - 1) - (pt + 1)>(lag);
      else if constexpr (pt >= 0 && (i == 3 || i == 7)) wait_vm<cnt2(pt - 1) - (pt + 1) + (u == NSTREAM - 1 && (WO_EARLY & 2) ? 6 : 0)>();
      if constexpr (pt >= 0 || (i != 3 && i != 7)) sfor<cnt2(pt - 1), cnt2(pt)>(issue2);
      if constexpr (i == 3) { Tile kt; read_k<s>(kt, addrK); if constexpr (pt < 0) sfor<cnt2(pt - 1), cnt2(pt)>(issue2); p = softmax_tile(st, qk(kt, Q)); }
      if constexpr (i == 7) { pv_tile<true>(st, p, VL[s], mk); if constexpr (pt < 0) sfor<cnt2(pt - 1), cnt2(pt)>(issue2); }
    });
  };
  using I0 = std::integral_constant<int, 0>;
  using I1 = std::integral_constant<int, 1>;

  auto warm = [&](int wi) {
    const v4i rsG = mk_rsrc(wg, DM * DFF * 2), rsU = mk_rsrc(wu, DM * DFF * 2), rsD = mk_rsrc(wd, DM * DFF * 2);
    const unsigned q = (b >> 3) * 1536u + tid + 256 * wi, r = q / 72u;
    const unsigned vg = r * (DFF * 2) + (q - r * 72u) * 16u;
    touch16(lbase + L_PAD, vg, rsG, (b & 7) * 1152u);
    touch16(lbase + L_PAD, vg, rsU, (b & 7) * 1152u);
    touch16(lbase + L_PAD, q < 41472u ? q * 16u : OOB, rsD, (b & 7) * (576u * DM * 2));
  };
  v4u wof[4], woc[2];
  auto load_wo = [&]() {
    const unsigned char* p = wo + (size_t)(b * 16 + 4 * g) * (DM * 2);
#pragma unroll
    for (int n = 0; n < 4; ++n) wof[n] = ld_w<(WDEF & 4) != 0>(p + n * (DM * 2) + 256 * w + 16 * c);
#pragma unroll
    for (int n = 0; n < 2; ++n) woc[n] = ld_w<(WDEF & 4) != 0>(p + (2 * n + (c >> 3)) * (DM * 2) + 1024 + 16 * (c & 7));
    asm volatile("" ::: "memory");
  };
  auto chain = [&](auto LAYER) {
    constexpr int layer = LAYER.value;
    constexpr bool dl_ = layer == DLAYER;
    relane();
    if constexpr (layer == 1) { while ((unsigned)__builtin_amdgcn_s_memrealtime() - t_in < FLOOR_Z) __builtin_amdgcn_s_sleep(1); }
    STAMP(3 + 10 * layer);
    const unsigned ph = 2 + 5 * layer;

    const int mu = !MUMAP ? (b & 7) * 18 + (b >> 3) : (b < 128 ? 16 * (b & 7) + (b >> 3) : (b < 136 ? 128 + 2 * (b & 7) : 129 + 2 * ((b + 4) & 7)));
    constexpr int GW = layer == 0 ? GATE_WIN : 27;
    v4u wb[GW];
    const unsigned char* pgu;
    unsigned odn, odc;
    auto bases = [&]() {
      pgu = (((c >> 2) & 1) ? wu : wg) + (size_t)(144 * w + (2 * g + (c >> 3)) * 18) * (DFF * 2) + 64 * mu + 16 * (c & 3);
      odn = (32 * mu + 8 * w + 2 * g) * (DM * 2) + 16 * c;
      odc = (32 * mu + 8 * w + 2 * g + (c >> 3)) * (DM * 2) + 1024 + 16 * (c & 7);
    };
    auto frag = [&](auto F) {
      constexpr int f = F.value;
      if constexpr (f < 18) wb[f % GW] = ld_w<(WDEF & 1) != 0>(pgu + (size_t)f * (DFF * 2));
      else if constexpr (f < 26) wb[f % GW] = ld_w<(WDEF & 2) != 0>(wd + odn + ((f - 18) & 1) * (DM * 2) + ((f - 18) >> 1) * 256);
      else if constexpr (f == 26) wb[f % GW] = ld_w<(WDEF & 2) != 0>(wd + odc);
    };
    constexpr int EF = layer == 0 ? EARLY_F0 : EARLY_F1;
    if (b < NMLP) { bases(); sfor<0, EF>(frag); }
    {
      const float lt = xrow_sum(st.l);
      float* cp = (float*)(smem + L_REDB) + w * 264;
#pragma unroll
      for (int r = 0; r < 4; ++r) cp[(c >> 3) * 128 + 32 * g + 8 * r + (c & 7)] = st.acc[r];
      if ((lane & 0x37) == 0) { f2 ml = {st.mc, lt}; *(f2*)(cp + 256 + 2 * (c >> 3)) = ml; }
    }
    wg_barrier();
    {
      const float* cp = (const float*)(smem + L_REDB);
      const int h = tid >> 7;
      f2 ml[4];
#pragma unroll
      for (int i = 0; i < 4; ++i) ml[i] = *(const f2*)(cp + i * 264 + 256 + 2 * h);
      const float M = fmaxf(fmaxf(ml[0][0], ml[1][0]), fmaxf(ml[2][0], ml[3][0]));
      float O = 0.f, L = 0.f;
#pragma unroll
      for (int i = 0; i < 4; ++i) { const float sc = ex2(ml[i][0] - M); O += sc * cp[i * 264 + tid]; L += sc * ml[i][1]; }
      const unsigned tg = TAG(ph);
      const unsigned pa = WS_PA + (head * 16 + (tid >> 4)) * PA_STRIDE + split * PA_MSG;
      v2u gr = {__float_as_uint(O), tg};
      __builtin_amdgcn_raw_buffer_store_b64(gr, rsW, pa + c * 8, 0, AUX_SC1);
      if (c == 0) {
        v4u g2 = {__float_as_uint(M), tg, __float_as_uint(L), tg};
        __builtin_amdgcn_raw_buffer_store_b128(g2, rsW, pa + 128, 0, AUX_SC1);
      }
    }
    STAMP(4 + 10 * layer);
    relane();
    {
      const unsigned tg = TAG(ph);
      const unsigned base = WS_PA + b * PA_STRIDE + c * PA_MSG;
      v4u a[3];
#pragma unroll
      for (int k = 0; k < 3; ++k) a[k] = (v4u){0u, 0u, 0u, 0u};
      if constexpr (DELAY_PA > 0) __builtin_amdgcn_s_sleep(DELAY_PA);
      POLL_BEGIN
        ldc3<16>(a, rsC, base + 32 * g, base + 128);
      POLL_END(tagged(a[0], tg) && tagged(a[1], tg) && tagged(a[2], tg), 26)
      STAMP(5 + 10 * layer);
      const float ms = __uint_as_float(a[2][0]);
      const float wgt = ex2(ms - row_max(ms));
      const float inv = frcp(row_sum(wgt * __uint_as_float(a[2][2])));
      float o[4];
      o[0] = row_sum(wgt * __uint_as_float(a[0][0])) * inv;
      o[1] = row_sum(wgt * __uint_as_float(a[0][2])) * inv;
      o[2] = row_sum(wgt * __uint_as_float(a[1][0])) * inv;
      o[3] = row_sum(wgt * __uint_as_float(a[1][2])) * inv;
      f4 d = mfma_first(wof[0], diag(pk_bf(o[0], o[0]), mk));
#pragma unroll
      for (int n = 1; n < 4; ++n) mfma_acc(d, wof[n], diag(pk_bf(o[n], o[n]), mk));
      mfma_fence(d);
      const unsigned tb = TAG(ph + 1);
      const bool hp = (c >> 3) != 0;
      auto put_line = [&](float lo, float hi, unsigned red, bool act) {
        const float lon = dpp<0xB1>(lo), hin = dpp<0xB1>(hi);
        const bool odd = (c & 1) != 0;
        v4u gr = {__float_as_uint(odd ? hin : lo), tb, __float_as_uint(odd ? hi : lon), tb};
        __builtin_amdgcn_raw_buffer_store_b128(gr, rsW, act ? WS_PB + red * PB_STRIDE + b * 128 + (odd ? 64 : 0) + (c & 6) * 8 : OOB, 0, AUX_SC1);
      };
      put_line(hp ? d[2] : d[0], hp ? d[3] : d[1], 8 * w + 2 * g + (hp ? 1 : 0), true);
      if (w == 0) {
        const float oa = hp ? o[1] : o[0], obv = hp ? o[3] : o[2];
        f4 e = mfma_first(woc[0], diag(pk_bf(oa, oa), mk));
        mfma_acc(e, woc[1], diag(pk_bf(obv, obv), mk));
        mfma_fence(e);
        float f[4];
#pragma unroll
        for (int r = 0; r < 4; ++r) {
          const float own = (g >> 1) == (hp ? 1 : 0) ? e[r] : 0.f;
          const float t = own + dpp<0x128>(own);
          auto sw = __builtin_amdgcn_permlane32_swap(__float_as_uint(t), __float_as_uint(t), false, false);
          f[r] = __uint_as_float(sw[0]) + __uint_as_float(sw[1]);
        }
        put_line(hp ? f[2] : f[0], hp ? f[3] : f[1], 32 + 2 * g + (hp ? 1 : 0), g < 2);
      }
    }
    STAMP(6 + 10 * layer);
    relane();
    if constexpr (layer == 0 && WARM2 > WARM1) {
#pragma unroll
      for (int i = WARM1; i < WARM2; ++i) warm(i);
      wait_vm<WARM_WAIT>();
    }
    v2u xres;
    auto reduce_cols = [&](auto NPc, unsigned tg) {
      constexpr int NP = NPc.value, NK = (NP * 8 + 255) / 256;
      v4u a[NK];
#pragma unroll
      for (int k = 0; k < NK; ++k) a[k] = (v4u){0u, 0u, 0u, 0u};
      static_assert(NK == 8 || NK == 5, "two shapes");
      const unsigned base = WS_PB + myr * PB_STRIDE;
      const bool off5 = tid + 1024 >= NP * 8;
      bool m;
      if constexpr (NK == 8 && DELAY_PB > 0) __builtin_amdgcn_s_sleep(DELAY_PB);
      POLL_BEGIN
        ldc4(a[0], a[1], a[2], a[3], rsC, tid * 16, base);
        if constexpr (NK == 8) { ldc4(a[4], a[5], a[6], a[7], rsC, tid * 16, base + 16384); pin4(a[4], a[5], a[6], a[7]); }
        else ldc1(a[4], rsC, off5 ? OOB : base + 16384 + tid * 16);
        pin4(a[0], a[1], a[2], a[3]);
        m = true;
#pragma unroll
        for (int k = 0; k < NK; ++k) m = m && (tagged(a[k], tg) || (NK == 5 && k == 4 && off5));
      POLL_END(m, NP == NWG ? 28 : 30)
      f2 s = {0.f, 0.f};
#pragma unroll
      for (int k = 0; k < NK; ++k) { s[0] += __uint_as_float(a[k][0]); s[1] += __uint_as_float(a[k][2]); }
#pragma unroll
      for (int i = 0; i < 2; ++i) { s[i] += dpp<0x128>(s[i]); s[i] = xrow_sum(s[i]); }
      if (lane < 8) ((f2*)(smem + L_REDA))[w * 8 + lane] = s;
      wg_barrier();
      const f4* rp = (const f4*)(smem + L_REDA) + (tid & 3);
      const f4 t = (rp[0] + rp[4]) + (rp[8] + rp[12]);
      if (w == 0) {
        const v2u xo = *(const v2u*)(smem + L_XR + (tid & 3) * 8);
        xres[0] = pk_bf(bf_lo(xo[0]) + rbf(t[0]), bf_hi(xo[0]) + rbf(t[1]));
        xres[1] = pk_bf(bf_lo(xo[1]) + rbf(t[2]), bf_hi(xo[1]) + rbf(t[3]));
        if (tid < 4) *(v2u*)(smem + L_XR + tid * 8) = xres;
      }
    };
    auto publish_x = [&](unsigned tg) {
      if (tid < 4 * XREP) {
        v4u gx = {xres[0], tg, xres[1], tg};
        __builtin_amdgcn_raw_buffer_store_b128(gx, rsW, WS_XG + (tid >> 2) * XG_STRIDE + myr * 64 + (tid & 3) * 16, 0, AUX_SC1);
      }
    };
    auto gather_x_ln = [&](unsigned tg) {
      v4u a[3];
#pragma unroll
      for (int k = 0; k < 3; ++k) a[k] = (v4u){0u, 0u, 0u, 0u};
      const unsigned xb = WS_XG + (b % XREP) * XG_STRIDE;
      POLL_BEGIN
        ldc3<1024>(a, rsC, xb + lane * 16, lane < 16 ? xb + 2048 + lane * 16 : OOB);
      POLL_END(tagged(a[0], tg) && tagged(a[1], tg) && (tagged(a[2], tg) || lane >= 16), tg == TAG(ph + 2) ? 28 : -1)
      v2u px[3];
#pragma unroll
      for (int k = 0; k < 3; ++k) { px[k][0] = a[k][0]; px[k][1] = a[k][2]; }
      layernorm(px);
    };

    if (b < NMLP) {
      bases();
      sfor<EF, GW>(frag);
      if (w == 3) gather_x_ln(TAG(ph + 2));
      relane();
      bases();
      wg_barrier();
      STAMP(8 + 10 * layer);
      f4 d;
      {
        const unsigned* xp = (const unsigned*)(smem + L_XB + (144 * w + (2 * g + (c >> 3)) * 18) * 2);
        unsigned xs[9];
        constexpr int XA = layer == 0 ? 4 : 9;
#pragma unroll
        for (int n = 0; n < XA; ++n) xs[n] = xp[n];
        sfor<0, 18>([&](auto Nn) {
          constexpr int n = Nn.value;
          const v4u bb = diag((n & 1) ? dup_hi(xs[n >> 1]) : dup_lo(xs[n >> 1]), mk);
          if constexpr ((n & 1) && (n >> 1) + XA < 9) xs[(n >> 1) + XA] = xp[(n >> 1) + XA];
          if constexpr (n == 0) d = mfma_first(wb[0], bb); else mfma_acc(d, wb[n % GW], bb);
          frag(std::integral_constant<int, n + GW>{});
          if constexpr (layer == 0) __builtin_amdgcn_sched_barrier(0);
        });
      }
      mfma_fence(d);
      if ((g >> 1) == (c >> 3)) {
        float* rp = (float*)(smem + L_REDA) + (w * 4 + g) * 32 + (c & 7);
#pragma unroll
        for (int r = 0; r < 4; ++r) rp[8 * r] = d[r];
      }
      wg_barrier();
      relane();
      unsigned hd0, hd1;
      const bool hp = (c >> 3) != 0;
      {
        f2 v = ((const f2*)(smem + L_REDA))[4 * w + g + 32 * (c & 7) + 16 * (c >> 3)];
        float hv[2];
#pragma unroll
        for (int i = 0; i < 2; ++i) {
          float t = v[i];
          t += dpp<0xB1>(t); t += dpp<0x4E>(t); t += dpp<0x141>(t);
          const float y = dpp<0x128>(t);
          const float x = rbf(hp ? y : t), up = rbf(hp ? t : y);
          const float z = 0.7978845608028654f * (x + 0.044715f * x * x * x);
          const float th = 1.f - 2.f * __builtin_amdgcn_rcpf(__expf(2.f * z) + 1.f);
          hv[i] = rbf(0.5f * x * (1.f + th)) * up;
        }
        hd0 = pk_bf(hv[0], hv[0]); hd1 = pk_bf(hv[1], hv[1]);
      }
      {
        const v4u b0 = diag(hd0, mk), b1 = diag(hd1, mk);
        float* rp = (float*)(smem + L_REDB);
        f4 e[5];
#pragma unroll
        for (int bl = 0; bl < 4; ++bl) {
          e[bl] = mfma_first(wb[(18 + 2 * bl) % GW], b0);
          mfma_acc(e[bl], wb[(19 + 2 * bl) % GW], b1);
        }
        e[4] = mfma_first(wb[26 % GW], diag(hp ? hd1 : hd0, mk));
        mfma_fence5(e);
#pragma unroll
        for (int bl = 0; bl < 4; ++bl) {
          float* q = rp + w * 512 + 128 * bl + 32 * g + (hp ? 16 : 0) + (c & 7);
          q[0] = hp ? e[bl][2] : e[bl][0]; q[8] = hp ? e[bl][3] : e[bl][1];
        }
        if ((g >> 1) == (c >> 3)) {
          float* q = rp + 2048 + w * 128 + 32 * g + (c & 7);
#pragma unroll
          for (int r = 0; r < 4; ++r) q[8 * r] = e[4][r];
        }
      }
      wg_barrier();
      {
        const f2* rp = (const f2*)(smem + L_REDB);
        const unsigned tg = TAG(ph + 3);
        const f2 a = (rp[tid] + rp[256 + tid]) + (rp[512 + tid] + rp[768 + tid]);
        v4u g0 = {__float_as_uint(a[0]), tg, __float_as_uint(a[1]), tg};
        __builtin_amdgcn_raw_buffer_store_b128(g0, rsW, WS_PB + (tid >> 3) * PB_STRIDE + mu * 128 + (tid & 7) * 16, 0, AUX_SC1);
        if (tid < 32) {
          f2 e = rp[1024 + tid];
#pragma unroll
          for (int i = 1; i < 8; ++i) e += rp[1024 + 32 * i + tid];
          v4u g1 = {__float_as_uint(e[0]), tg, __float_as_uint(e[1]), tg};
          __builtin_amdgcn_raw_buffer_store_b128(g1, rsW, WS_PB + (32 + (tid >> 3)) * PB_STRIDE + mu * 128 + (tid & 7) * 16, 0, AUX_SC1);
        }
      }
      STAMP(9 + 10 * layer);
      if constexpr (layer == 0) issue_wq();
    } else {
      if (myr >= 0) {
        reduce_cols(std::integral_constant<int, NWG>{}, TAG(ph + 1));
        publish_x(TAG(ph + 2));
        STAMP(7 + 10 * layer);
      }
      if constexpr (layer == 0) issue_wq();
      if (myr >= 0) {
        reduce_cols(std::integral_constant<int, NMLP>{}, TAG(ph + 3));
        if constexpr (layer == 0) publish_x(TAG(ph + 4));
        else if (tid < 4) {
          while ((unsigned)__builtin_amdgcn_s_memrealtime() - t_in < FLOOR_Y) __builtin_amdgcn_s_sleep(1);
          *(v2u*)(out + 16 * myr + 4 * tid) = xres;
          if (myr == 0 && tid == 0) __builtin_amdgcn_raw_buffer_store_b32(tagbase + 16u, rsW, WS_EPOCH, 0, AUX_SC1);
        }
        STAMP(10 + 10 * layer);
      }
    }
    if constexpr (layer == 0) {
      relane();
      if (w == 3) gather_x_ln(TAG(ph + 4));
      wg_barrier();
      STAMP(25);
      wait_wq<0>(wqf);
      STAMP(21);
      q_gemv();
      STAMP(22);
      wg_barrier();
      if (w == 3) q_exchange(TAG(ph + 5));
      wg_barrier();
      STAMP(11);
    }
  };

  load_q();
  sfor<0, T_RING>(step1);
  STAMP(2);
  sfor<0, 4 * RS>([&](auto J) { if constexpr (8 * (J.value >> 2) + 4 + (J.value & 3) >= UF) def_a(VL[J.value >> 2].r[J.value & 3]); });
#pragma unroll 1
  for (int t = T_RING; t < T_TAIL; t += RS) sfor<0, RS>([&](auto S) { ring_step(S, t + S.value, PACE1); });
  sfor<T_TAIL, NTILE - 1>(step1);
  if constexpr (WO_EARLY & 1) load_wo();
#pragma unroll
  for (int i = 0; i < WARM1; ++i) warm(i);
  step1(std::integral_constant<int, NTILE - 1>{});
  if constexpr (!(WO_EARLY & 1)) load_wo();
  chain(I0{});
  relane();
  while ((unsigned)__builtin_amdgcn_s_memrealtime() - t_in < FLOOR_X) __builtin_amdgcn_s_sleep(1);
  if constexpr (PACE != 0) due = now8() + PACE_OFF2 * 256;
  load_q();
  static_assert(NKEEP % RS == 0 && NSTREAM % RS == 0, "ring parity");
  sfor<0, NKEEP>([&](auto J) {
    constexpr int i = J.value;
    step2(std::integral_constant<int, i - RS>{});
    if constexpr (i < NR) {
      const f4 p = softmax_tile(st, qk(KR[i], Q));
      pv_tile<vr_in_a(i)>(st, p, VR[i], mk);
    } else {
      Tile kt;
      read_k<RS + (i - NR)>(kt, addrK);
      const f4 p = softmax_tile(st, qk(kt, Q));
      pv_tile<true>(st, p, VL[RS + (i - NR)], mk);
    }
  });
  STAMP(12);
#pragma unroll 1
  for (int u = NKEEP - RS; u < NSTREAM - RS; u += RS) sfor<0, RS>([&](auto S) { ring_step(S, T_RING + u + S.value - NPARK, PACE); });
  step2(std::integral_constant<int, NSTREAM - 2>{});
  if constexpr (WO_EARLY & 2) load_wo();
  step2(std::integral_constant<int, NSTREAM - 1>{});
  if constexpr (!(WO_EARLY & 2)) load_wo();
  chain(I1{});
  asm volatile(".Lfd_code_to:");
}

}

extern "C" void kernel_launch(void* const* d_in, const int* in_sizes, int n_in, void* d_out, int out_size, void* d_ws, size_t ws_size,
                              hipStream_t stream) {
  (void)in_sizes; (void)n_in; (void)out_size; (void)ws_size;
  fused_decode_kernel<<<NWG, 256, 0, stream>>>(
      (const unsigned short*)d_in[0], (const unsigned char*)d_in[1], (const unsigned char*)d_in[2], (const unsigned char*)d_in[3],
      (const unsigned char*)d_in[4], (const unsigned char*)d_in[6], (const unsigned char*)d_in[7], (const unsigned char*)d_in[8],
      (unsigned short*)d_out, (unsigned char*)d_ws);
}
```

```cpp
#include <hip/hip_runtime.h>
#include <hip/hip_bf16.h>
#include <type_traits>

namespace {

typedef unsigned v4u __attribute__((ext_vector_type(4)));
typedef unsigned v2u __attribute__((ext_vector_type(2)));
typedef int v4i __attribute__((ext_vector_type(4)));
typedef float f4 __attribute__((ext_vector_type(4)));
typedef float f2 __attribute__((ext_vector_type(2)));
typedef __bf16 bf2 __attribute__((ext_vector_type(2)));
#define FI __device__ __forceinline__

constexpr int DM = 576, DFF = 4608, NQ = 4096;
constexpr int NWG = 256;
constexpr int NMLP = 144;
constexpr int NRED = 36;
constexpr int XREP = 8;
constexpr int NTILE = 64;
constexpr int NR = 9;
constexpr int NL = 9;
constexpr int D = 3;
constexpr int NSTREAM = NTILE - NR - NL, NTA = 10, NB1 = 2, NT1 = NSTREAM / NB1, NB2 = 2, NT2 = (NSTREAM - NTA) / NB2;
static_assert(NB1 * NT1 == NSTREAM && NTA + NB2 * NT2 == NSTREAM && D >= 2 && D <= 4 && NR + NL - D <= 2 * NTA && NT1 <= 32 && NT2 <= 32, "schedule");
static_assert(NT1 * 256 <= (NL - D) * 4096 && 2 * NT1 <= (NL - D) * 16, "room for the rescale factors and the parked probabilities");
static_assert(4 * XREP <= 64, "wave 0 writes every copy of x");
constexpr int GATE_WIN = 12;
constexpr int WARM1 = 2;
constexpr int QG_AT = 5;
constexpr unsigned PACE_R = 14208, PACE_B1 = 13888, PACE_A2 = 14208, PACE_B2 = 14080;
constexpr int PACE_OFF1 = -10, TB_DELTA = 40, PACE_OFF2 = 180, SLACK2 = 40;
static_assert(GATE_WIN >= 9, "all down fragments must fit the window");
constexpr float QSCALE = 0.08838834764831845f * 1.4426950408889634f;
constexpr unsigned SPIN_CAP = 1u << 21;

constexpr unsigned WS_EPOCH = 0;
constexpr unsigned WS_TB = 256;
constexpr unsigned WS_QG = 4096;
constexpr unsigned WS_XG = 32768, XG_STRIDE = 2560;
constexpr unsigned WS_PA = 65536 + 32768, PA_MSG = 160, PA_STRIDE = 16 * PA_MSG;
constexpr unsigned WS_PB = 1u << 20, PB_STRIDE = NWG * 128;
constexpr unsigned WS_BYTES = WS_PB + NRED * PB_STRIDE;
constexpr unsigned OOB = 0x7fffff00u;

constexpr int L_KS = 0;
constexpr int L_SC = 4 * NL * 4096;
constexpr int L_REDB = L_SC;
constexpr int L_REDA = L_SC + 10240;
constexpr int L_XB = L_REDA + 2048;
constexpr int L_QB = L_XB + 1280;
constexpr int L_PAD = L_QB + 512;
constexpr int L_FLAG = L_PAD + 1024;
constexpr int L_XR = L_FLAG + 32;
constexpr int L_BYTES = L_XR + 32;
static_assert(L_BYTES <= 163840, "LDS");

template <int B, int E, class F> FI void sfor(F&& f) {
  if constexpr (B < E) { f(std::integral_constant<int, B>{}); sfor<B + 1, E>(f); }
}

FI v4i mk_rsrc(const void* p, unsigned bytes) {
  unsigned long long a = (unsigned long long)p;
  v4i r = {(int)(unsigned)a, (int)((unsigned)(a >> 32) & 0xffffu), (int)bytes, 0x00020000};
  return r;
}
FI float bf_lo(unsigned u) { return __uint_as_float(u << 16); }
FI float bf_hi(unsigned u) { return __uint_as_float(u & 0xffff0000u); }
FI unsigned pk_bf(float a, float b) { f2 v = {a, b}; return __builtin_bit_cast(unsigned, __builtin_convertvector(v, bf2)); }
FI float rbf(float a) { return bf_lo(pk_bf(a, a)); }
FI float frcp(float x) { const float r = __builtin_amdgcn_rcpf(x); return __builtin_fmaf(__builtin_fmaf(-x, r, 1.f), r, r); }
FI float ex2(float x) { return __builtin_amdgcn_exp2f(x); }

template <int CTRL> FI float dpp(float x) {
  return __builtin_bit_cast(float, __builtin_amdgcn_mov_dpp(__builtin_bit_cast(int, x), CTRL, 0xf, 0xf, true));
}
FI float row_sum(float x) {
  x += dpp<0xB1>(x); x += dpp<0x4E>(x); x += dpp<0x141>(x); x += dpp<0x128>(x); return x;
}
FI float row_max(float x) {
  x = fmaxf(x, dpp<0xB1>(x)); x = fmaxf(x, dpp<0x4E>(x)); x = fmaxf(x, dpp<0x141>(x)); x = fmaxf(x, dpp<0x128>(x)); return x;
}
FI float xrow_max(float x) {
  auto s = __builtin_amdgcn_permlane16_swap(__float_as_uint(x), __float_as_uint(x), false, false);
  x = fmaxf(__uint_as_float(s[0]), __uint_as_float(s[1]));
  auto t = __builtin_amdgcn_permlane32_swap(__float_as_uint(x), __float_as_uint(x), false, false);
  return fmaxf(__uint_as_float(t[0]), __uint_as_float(t[1]));
}
FI float xrow_sum(float x) {
  auto s = __builtin_amdgcn_permlane16_swap(__float_as_uint(x), __float_as_uint(x), false, false);
  x = __uint_as_float(s[0]) + __uint_as_float(s[1]);
  auto t = __builtin_amdgcn_permlane32_swap(__float_as_uint(x), __float_as_uint(x), false, false);
  return __uint_as_float(t[0]) + __uint_as_float(t[1]);
}
FI float wave_sum(float x) { return xrow_sum(row_sum(x)); }
template <int N> FI void row_sums(float (&x)[N]) {
#pragma unroll
  for (int i = 0; i < N; ++i) x[i] += dpp<0xB1>(x[i]);
  __builtin_amdgcn_sched_barrier(0);
#pragma unroll
  for (int i = 0; i < N; ++i) x[i] += dpp<0x4E>(x[i]);
  __builtin_amdgcn_sched_barrier(0);
#pragma unroll
  for (int i = 0; i < N; ++i) x[i] += dpp<0x141>(x[i]);
  __builtin_amdgcn_sched_barrier(0);
#pragma unroll
  for (int i = 0; i < N; ++i) x[i] += dpp<0x128>(x[i]);
  __builtin_amdgcn_sched_barrier(0);
}
FI void xrow_sums2(float& a, float& b) {
  auto s = __builtin_amdgcn_permlane16_swap(__float_as_uint(a), __float_as_uint(b), false, false);
  const float t = __uint_as_float(s[0]) + __uint_as_float(s[1]);
  auto u = __builtin_amdgcn_permlane32_swap(__float_as_uint(t), __float_as_uint(t), false, false);
  const float v = __uint_as_float(u[0]) + __uint_as_float(u[1]);
  auto r = __builtin_amdgcn_permlane16_swap(__float_as_uint(v), __float_as_uint(v), false, false);
  a = __uint_as_float(r[0]); b = __uint_as_float(r[1]);
}
FI void pin(unsigned& a) { asm volatile("" : "+v"(a)); }

FI void wg_barrier() { asm volatile("s_waitcnt lgkmcnt(0)\n\ts_barrier" ::: "memory"); }
template <int N> FI void wait_vm() { asm volatile("s_waitcnt vmcnt(%0)" ::"i"(N > 63 ? 63 : N) : "memory"); }

FI void mfma_acc(f4& d, const v4u& a, const v4u& b) {
  asm("s_nop 1\n\tv_mfma_f32_16x16x32_bf16 %0, %1, %2, %0" : "+v"(d) : "v"(a), "v"(b));
}
FI f4 mfma_first(const v4u& a, const v4u& b) {
  f4 d;
  asm("s_nop 1\n\tv_mfma_f32_16x16x32_bf16 %0, %1, %2, 0" : "=&v"(d) : "v"(a), "v"(b));
  return d;
}
FI void mfma_acc2(f4& d, const v4u& a0, const v4u& b0, const v4u& a1, const v4u& b1) {
  asm("s_nop 1\n\tv_mfma_f32_16x16x32_bf16 %0, %1, %2, %0\n\tv_mfma_f32_16x16x32_bf16 %0, %3, %4, %0" : "+v"(d) : "v"(a0), "v"(b0), "v"(a1), "v"(b1));
}
FI f4 mfma_first2(const v4u& a0, const v4u& b0, const v4u& a1, const v4u& b1) {
  f4 d;
  asm("s_nop 1\n\tv_mfma_f32_16x16x32_bf16 %0, %1, %2, 0\n\tv_mfma_f32_16x16x32_bf16 %0, %3, %4, %0" : "=&v"(d) : "v"(a0), "v"(b0), "v"(a1), "v"(b1));
  return d;
}
FI void mfma_fence(f4& d) { asm("s_nop 7\n\ts_nop 1" : "+v"(d)); }

FI void mfma_fence2(f4& d, f4& e) { asm("s_nop 7\n\ts_nop 1" : "+v"(d), "+v"(e)); }
FI void mfma_fence5(f4 (&e)[5]) { asm("s_nop 7\n\ts_nop 1" : "+v"(e[0]), "+v"(e[1]), "+v"(e[2]), "+v"(e[3]), "+v"(e[4])); }

struct Masks { unsigned m0, m1, m2, m3; };
FI v4u diag(unsigned dup, const Masks& k) { v4u b = {dup & k.m0, dup & k.m1, dup & k.m2, dup & k.m3}; return b; }
FI void pin(Masks& k) { asm volatile("" : "+v"(k.m0), "+v"(k.m1), "+v"(k.m2), "+v"(k.m3)); }
FI unsigned dup_lo(unsigned x) { return __builtin_amdgcn_perm(x, x, 0x01000100u); }
FI unsigned dup_hi(unsigned x) { return __builtin_amdgcn_perm(x, x, 0x03020302u); }

struct Tile { v4u r[4]; };
struct WoFrag { v4u f[4], c[2]; };

struct Ln {
  int lane, g, c;
  Masks mk;
};
FI Ln make_ln() {
  Ln L;
  asm volatile("v_mbcnt_lo_u32_b32 %0, -1, 0\n\tv_mbcnt_hi_u32_b32 %0, -1, %0" : "=v"(L.lane));
  L.g = L.lane >> 4; L.c = L.lane & 15;
  const unsigned hm = (L.c & 1) ? 0xffff0000u : 0x0000ffffu;
  const int e2 = (L.c & 7) >> 1;
  L.mk.m0 = e2 == 0 ? hm : 0u; L.mk.m1 = e2 == 1 ? hm : 0u; L.mk.m2 = e2 == 2 ? hm : 0u; L.mk.m3 = e2 == 3 ? hm : 0u;
  return L;
}

FI void issue_k(unsigned m0v, unsigned voff, const v4i& rs, unsigned soff) {
  unsigned keep, tmp;
  asm volatile(
      "s_nop 1\n\t"
      "s_mov_b32 %0, m0\n\t"
      "s_mov_b32 m0, %2\n\t"
      "v_xor_b32 %1, 64, %3\n\t"
      "buffer_load_dwordx4 %3, %4, %5 offen nt lds\n\t"
      "buffer_load_dwordx4 %1, %4, %5 offen offset:1024 nt lds\n\t"
      "v_xor_b32 %1, 0x80, %3\n\t"
      "buffer_load_dwordx4 %1, %4, %5 offen offset:2048 nt lds\n\t"
      "v_xor_b32 %1, 0xc0, %3\n\t"
      "buffer_load_dwordx4 %1, %4, %5 offen offset:3072 nt lds\n\t"
      "s_mov_b32 m0, %0"
      : "=&s"(keep), "=&v"(tmp)
      : "s"(m0v), "v"(voff), "s"(rs), "s"(soff)
      : "memory");
}
#define V_LOADS \
  "s_nop 4\n\t" \
  "buffer_load_dwordx4 %0, %4, %5, %6 offen nt\n\t" \
  "buffer_load_dwordx4 %1, %4, %5, %6 offen offset:1024 nt\n\t" \
  "buffer_load_dwordx4 %2, %4, %5, %6 offen offset:2048 nt\n\t" \
  "buffer_load_dwordx4 %3, %4, %5, %6 offen offset:3072 nt"
template <int MODE> FI void issue_v(Tile& t, unsigned voff, const v4i& rs, unsigned soff) {
  if constexpr (MODE == 0)
    asm volatile(V_LOADS : "=a"(t.r[0]), "=a"(t.r[1]), "=a"(t.r[2]), "=a"(t.r[3]) : "v"(voff), "s"(rs), "s"(soff) : "memory");
  else if constexpr (MODE == 1)
    asm volatile(V_LOADS : "=&v"(t.r[0]), "=&v"(t.r[1]), "=&v"(t.r[2]), "=&v"(t.r[3]) : "v"(voff), "s"(rs), "s"(soff) : "memory");
  else
    asm volatile(V_LOADS ::"a"(t.r[0]), "a"(t.r[1]), "a"(t.r[2]), "a"(t.r[3]), "v"(voff), "s"(rs), "s"(soff) : "memory");
}
FI void def_a(Tile& t) { asm volatile("" : "=a"(t.r[0]), "=a"(t.r[1]), "=a"(t.r[2]), "=a"(t.r[3])); }
template <bool A> FI unsigned park(unsigned v) {
  if constexpr (A) { unsigned r; asm("v_accvgpr_write_b32 %0, %1" : "=a"(r) : "v"(v)); return r; } else return v;
}
template <bool A> FI unsigned unpark(unsigned r) {
  if constexpr (A) { unsigned v; asm("v_accvgpr_read_b32 %0, %1" : "=v"(v) : "a"(r)); return v; } else return r;
}
FI void touch(unsigned m0v, unsigned voff, const v4i& rs, unsigned soff) {
  unsigned keep;
  asm volatile(
      "s_mov_b32 %0, m0\n\t"
      "s_mov_b32 m0, %1\n\t"
      "s_nop 2\n\t"
      "buffer_load_dword %2, %3, %4 offen lds\n\t"
      "s_mov_b32 m0, %0"
      : "=&s"(keep)
      : "s"(m0v), "v"(voff), "s"(rs), "s"(soff)
      : "memory");
}
FI void touch16(unsigned m0v, unsigned voff, const v4i& rs, unsigned soff) {
  unsigned keep;
  asm volatile(
      "s_mov_b32 %0, m0\n\t"
      "s_mov_b32 m0, %1\n\t"
      "s_nop 2\n\t"
      "buffer_load_dwordx4 %2, %3, %4 offen lds\n\t"
      "s_mov_b32 m0, %0"
      : "=&s"(keep)
      : "s"(m0v), "v"(voff), "s"(rs), "s"(soff)
      : "memory");
}
FI void ride_load(const v4u& q, const v4i& rs, unsigned voff) {
  asm volatile("s_nop 4\n\tbuffer_load_dwordx4 %0, %1, %2, 0 offen sc1" ::"v"(q), "v"(voff), "s"(rs) : "memory");
}
FI v4u ride_take(const v4u& q) {
  v4u r;
  asm volatile("v_mov_b64 %0, %2\n\tv_mov_b64 %1, %3" : "=&v"(*(v2u*)&r), "=&v"(*((v2u*)&r + 1)) : "v"(*(const v2u*)&q), "v"(*((const v2u*)&q + 1)));
  return r;
}
FI void def_v(v4u& t) { asm volatile("" : "=v"(t)); }
FI void ldm3(v4u (&a)[3], const v4i& rs, unsigned v0, unsigned v1, unsigned v2, unsigned long long m) {
  unsigned long long keep;
  asm volatile(
      "s_mov_b64 %3, exec\n\t"
      "s_mov_b64 exec, %8\n\t"
      "s_nop 2\n\t"
      "buffer_load_dwordx4 %0, %5, %4, 0 offen sc1\n\t"
      "buffer_load_dwordx4 %1, %6, %4, 0 offen sc1\n\t"
      "buffer_load_dwordx4 %2, %7, %4, 0 offen sc1\n\t"
      "s_mov_b64 exec, %3\n\t"
      "s_waitcnt vmcnt(0)"
      : "+v"(a[0]), "+v"(a[1]), "+v"(a[2]), "=&s"(keep) : "s"(rs), "v"(v0), "v"(v1), "v"(v2), "s"(m) : "memory");
}
FI unsigned long long untagged(const v4u& a, unsigned tg) {
  return __builtin_amdgcn_ballot_w64(a[1] != tg) | __builtin_amdgcn_ballot_w64(a[3] != tg);
}
FI void poll3(v4u (&a)[3], const v4i& rs, unsigned v0, unsigned v1, unsigned v2, unsigned tg, unsigned long long care2) {
#pragma unroll
  for (int k = 0; k < 3; ++k) a[k] = (v4u){0u, 0u, 0u, 0u};
  unsigned long long miss = ~0ull;
#pragma unroll 1
  for (unsigned spins = 0; spins < SPIN_CAP; ++spins) {
    ldm3(a, rs, v0, v1, v2, miss);
    miss &= untagged(a[0], tg) | untagged(a[1], tg) | (untagged(a[2], tg) & care2);
    if (miss == 0ull) break;
  }
}
#define LDM(i, m) "s_mov_b64 exec, %" #m "\n\tbuffer_load_dwordx4 %" #i ", %7, %6, %5 offen sc1\n\ts_add_u32 %5, %5, 0x1000\n\t"
FI void ldm4(v4u& a0, v4u& a1, v4u& a2, v4u& a3, const v4i& rs, unsigned v0, unsigned so, unsigned long long m0, unsigned long long m1,
             unsigned long long m2, unsigned long long m3) {
  unsigned long long keep;
  asm volatile("s_mov_b64 %4, exec\n\ts_nop 3\n\t" LDM(0, 8) LDM(1, 9) LDM(2, 10) LDM(3, 11) "s_mov_b64 exec, %4"
               : "+v"(a0), "+v"(a1), "+v"(a2), "+v"(a3), "=&s"(keep), "+s"(so)
               : "s"(rs), "v"(v0), "s"(m0), "s"(m1), "s"(m2), "s"(m3)
               : "memory", "scc");
}
FI void pin4(v4u& a0, v4u& a1, v4u& a2, v4u& a3) { asm volatile("s_waitcnt vmcnt(0)" : "+v"(a0), "+v"(a1), "+v"(a2), "+v"(a3) :: "memory"); }
FI void lds_put(unsigned addr, unsigned v) { asm volatile("s_waitcnt lgkmcnt(0)\n\tds_write_b32 %0, %1" ::"v"(addr), "v"(v) : "memory"); }
FI unsigned lds_get(unsigned addr) {
  unsigned v;
  asm volatile("ds_read_b32 %0, %1\n\ts_waitcnt lgkmcnt(0)" : "=v"(v) : "v"(addr) : "memory");
  return __builtin_amdgcn_readfirstlane(v);
}
template <bool NT = false> FI v4u ld_w(const unsigned char* p) { if constexpr (NT) return __builtin_nontemporal_load((const v4u*)p); else return *(const v4u*)p; }
template <int SLOT> FI void read_k(Tile& k, unsigned addr) {
  unsigned tmp;
  asm volatile(
      "ds_read_b128 %0, %5 offset:%6\n\t"
      "v_xor_b32 %4, 16, %5\n\t"
      "ds_read_b128 %1, %4 offset:%6\n\t"
      "v_xor_b32 %4, 32, %5\n\t"
      "ds_read_b128 %2, %4 offset:%6\n\t"
      "v_xor_b32 %4, 48, %5\n\t"
      "ds_read_b128 %3, %4 offset:%6\n\t"
      "s_waitcnt lgkmcnt(0)"
      : "=&v"(k.r[0]), "=&v"(k.r[1]), "=&v"(k.r[2]), "=&v"(k.r[3]), "=&v"(tmp)
      : "v"(addr), "i"(SLOT * 4096)
      : "memory");
}
FI f4 qk(const Tile& k, const Tile& q) {
  f4 s;
  asm volatile(
      "v_mfma_f32_16x16x32_bf16 %0, %1, %5, 0\n\t"
      "v_mfma_f32_16x16x32_bf16 %0, %2, %6, %0\n\t"
      "v_mfma_f32_16x16x32_bf16 %0, %3, %7, %0\n\t"
      "v_mfma_f32_16x16x32_bf16 %0, %4, %8, %0\n\t"
      "s_nop 7\n\ts_nop 1"
      : "=&v"(s)
      : "v"(k.r[0]), "v"(k.r[1]), "v"(k.r[2]), "v"(k.r[3]), "v"(q.r[0]), "v"(q.r[1]), "v"(q.r[2]), "v"(q.r[3]));
  return s;
}
FI void load_b64(v2u& d, unsigned voff, const v4i& rs, unsigned soff) {
  asm volatile("s_nop 4\n\tbuffer_load_dwordx2 %0, %1, %2, %3 offen nt" : "=v"(d) : "v"(voff), "s"(rs), "s"(soff) : "memory");
}
template <int N> FI void wait_wq(v2u (&q)[12]) {
  asm volatile("s_waitcnt vmcnt(%12)"
               : "+v"(q[0]), "+v"(q[1]), "+v"(q[2]), "+v"(q[3]), "+v"(q[4]), "+v"(q[5]), "+v"(q[6]), "+v"(q[7]), "+v"(q[8]), "+v"(q[9]),
                 "+v"(q[10]), "+v"(q[11])
               : "i"(N) : "memory");
}

struct Soft { f4 acc; float mc, l; };

template <bool LATER> FI v2u softmax_tile(Soft& st, const f4& s, float* slot, unsigned& moved, unsigned bit) {
  f4 t;
#pragma unroll
  for (int r = 0; r < 4; ++r) t[r] = __builtin_fmaf(s[r], QSCALE, -st.mc);
  const float tm = fmaxf(fmaxf(t[0], t[1]), fmaxf(t[2], t[3]));
  if (__builtin_amdgcn_ballot_w64(tm > 0.f) != 0ull) {
    const float sm = fmaxf(fmaxf(s[0], s[1]), fmaxf(s[2], s[3])) * QSCALE;
    const float mn = fmaxf(st.mc, xrow_max(sm));
    const float al = ex2(st.mc - mn);
    st.mc = mn;
    st.l *= al;
    if constexpr (LATER) { *slot = al; moved |= bit; } else st.acc *= al;
#pragma unroll
    for (int r = 0; r < 4; ++r) t[r] = __builtin_fmaf(s[r], QSCALE, -mn);
  }
  f4 p;
#pragma unroll
  for (int r = 0; r < 4; ++r) p[r] = ex2(t[r]);
  st.l += (p[0] + p[1]) + (p[2] + p[3]);
  v2u pp = {pk_bf(p[0], p[1]), pk_bf(p[2], p[3])};
  return pp;
}
#define PV_MFMAS \
  "s_nop 1\n\t" \
  "v_mfma_f32_16x16x32_bf16 %0, %1, %5, %0\n\t" \
  "v_mfma_f32_16x16x32_bf16 %0, %2, %6, %0\n\t" \
  "v_mfma_f32_16x16x32_bf16 %0, %3, %7, %0\n\t" \
  "v_mfma_f32_16x16x32_bf16 %0, %4, %8, %0\n\t" \
  "s_nop 7\n\ts_nop 1"
template <bool A> FI void pv_tile(Soft& st, const v2u& p, const Tile& v, const Masks& k) {
  const v4u b0 = diag(dup_lo(p[0]), k), b1 = diag(dup_hi(p[0]), k), b2 = diag(dup_lo(p[1]), k), b3 = diag(dup_hi(p[1]), k);
  if constexpr (A)
    asm volatile(PV_MFMAS : "+v"(st.acc) : "a"(v.r[0]), "a"(v.r[1]), "a"(v.r[2]), "a"(v.r[3]), "v"(b0), "v"(b1), "v"(b2), "v"(b3));
  else
    asm volatile(PV_MFMAS : "+v"(st.acc) : "v"(v.r[0]), "v"(v.r[1]), "v"(v.r[2]), "v"(v.r[3]), "v"(b0), "v"(b1), "v"(b2), "v"(b3));
}

constexpr bool vr_in_a(int i) { return i < 7; }

constexpr int imin(int a, int b) { return a < b ? a : b; }
constexpr int imax(int a, int b) { return a > b ? a : b; }

constexpr int AUX_SC1 = 16;

__global__ void __launch_bounds__(256, 1)
fused_decode_kernel(const unsigned short* __restrict__ xin, const unsigned char* __restrict__ kc, const unsigned char* __restrict__ vc,
                    const unsigned char* __restrict__ wq, const unsigned char* __restrict__ wo, const unsigned char* __restrict__ wg,
                    const unsigned char* __restrict__ wu, const unsigned char* __restrict__ wd, unsigned short* __restrict__ out,
                    unsigned char* ws) {
  __shared__ __attribute__((aligned(16))) unsigned char smem[L_BYTES];
  const int b = blockIdx.x;
  const int w = __builtin_amdgcn_readfirstlane(threadIdx.x >> 6);
  const int head = b >> 4, split = b & 15;
  const int myr = b - (NWG - NRED);
  const unsigned lbase = (unsigned)(size_t)smem;

  const v4i rsK = mk_rsrc(kc + ((size_t)head << 24), 1u << 24);
  const v4i rsV = mk_rsrc(vc + ((size_t)head << 24), 1u << 24);
  const v4i rsQ = mk_rsrc(wq, DM * NQ * 2);
  const v4i rsC = mk_rsrc(ws, WS_BYTES);
  const __amdgpu_buffer_rsrc_t rsW = __builtin_amdgcn_make_buffer_rsrc(ws, 0, WS_BYTES, 0x00020000);

  const unsigned m0base = lbase + L_KS + w * (NL * 4096);
  const unsigned soffw = split * 16384 + w * 4096;
  auto toff = [&](unsigned t) { return soffw + (t << 18); };
  int lane, g, c, tid;
  Masks mk;
  unsigned voffV, voffK, addrK;
  auto relane = [&]() {
    const Ln L = make_ln();
    lane = L.lane; g = L.g; c = L.c; mk = L.mk; tid = w * 64 + lane;
    voffV = lane * 16;
    voffK = g * 256 + ((c ^ g) << 4);
    const unsigned kap = 4 * (c & 3) + (c >> 2);
    addrK = m0base + kap * 256 + (((4 * g) ^ kap) << 4);
  };
  relane();

  Tile KR[NR], VR[NR], VL[NL];

  auto now8 = [&]() { return (unsigned)__builtin_amdgcn_s_memrealtime() << 8; };
  unsigned t_go = 0, due = 0;
  auto gate = [&](unsigned pace) {
    due += pace;
    while ((int)(now8() - due) < 0) __builtin_amdgcn_s_sleep(1);
  };
  auto send_k = [&](int slot, unsigned t) { issue_k(m0base + slot * 4096, voffK, rsK, toff(t)); };

  const int qs = (b & 7) * 32 + (b >> 3);
  v2u wqf[12];
  auto issue_wq = [&]() {
    const unsigned voffQ = (192u * w + (4 * g + (c >> 2)) * 12u) * (NQ * 2) + (unsigned)(qs * 16 + 4 * (c & 3)) * 2u;
    sfor<0, 12>([&](auto I) {
      load_b64(wqf[I.value], voffQ, rsQ, I.value * NQ * 2);
    });
  };

  auto xpiece = [&](int np, int k) { const unsigned j = np * w + lane + 64 * k; return j >= 144u ? j - 144u : j; };
  auto layernorm = [&](v2u (&px)[3], int np) {
    const bool act2 = lane < 16;
    auto up = [](unsigned pr) { return (f2){bf_lo(pr), bf_hi(pr)}; };
    const float x0 = bf_lo(__builtin_amdgcn_readfirstlane(px[0][0]));
    f2 s = {0.f, 0.f}, q = {0.f, 0.f};
#pragma unroll
    for (int k = 0; k < 3; ++k) {
#pragma unroll
      for (int i = 0; i < 2; ++i) {
        f2 a = up(px[k][i]) - x0;
        if (k == 2) a = act2 ? a : (f2){0.f, 0.f};
        s += a;
        q = __builtin_elementwise_fma(a, a, q);
      }
    }
    float sq[2] = {s[0] + s[1], q[0] + q[1]};
    row_sums(sq);
    xrow_sums2(sq[0], sq[1]);
    const float md = sq[0] * (1.f / DM);
    const float mean = x0 + md;
    const float var = fmaxf(__builtin_fmaf(-md, md, sq[1] * (1.f / DM)), 0.f);
    const float mb = rbf(mean);
    const float den = rbf(__builtin_amdgcn_sqrtf(rbf(rbf(var) + 1e-5f)));
    const float rc = __builtin_amdgcn_rcpf(den);
    auto dv = [&](unsigned pr) {
      const unsigned a2 = pk_bf(bf_lo(pr) - mb, bf_hi(pr) - mb);
      const f2 a = {bf_lo(a2), bf_hi(a2)};
      const f2 q0 = a * rc;
      const f2 q1 = __builtin_elementwise_fma(__builtin_elementwise_fma(-q0, (f2){den, den}, a), (f2){rc, rc}, q0);
      return pk_bf(q1[0], q1[1]);
    };
    v2u o = {dv(px[0][0]), dv(px[0][1])};
    if (lane < np) *(v2u*)(smem + L_XB + (np * w + lane) * 8) = o;
  };

  auto q_gemv = [&]() {
    if (w < 3) {
      const v2u* xp = (const v2u*)(smem + L_XB + (192 * w + (4 * g + (c >> 2)) * 12) * 2);
      const v2u xa = xp[0], xb = xp[1], xc = xp[2];
      const unsigned xs[6] = {xa[0], xa[1], xb[0], xb[1], xc[0], xc[1]};
      const unsigned ma = mk.m0 | mk.m2, mb = mk.m1 | mk.m3;
      f4 d;
#pragma unroll
      for (int n = 0; n < 6; ++n) {
        const unsigned lo = dup_lo(xs[n]), hi = dup_hi(xs[n]);
        const v4u bb = {lo & ma, lo & mb, hi & ma, hi & mb};
        const v4u aa = {wqf[2 * n][0], wqf[2 * n][1], wqf[2 * n + 1][0], wqf[2 * n + 1][1]};
        if (n == 0) d = mfma_first(aa, bb); else mfma_acc(d, aa, bb);
      }
      mfma_fence(d);
      if ((c >> 2) == g) {
        float* rp = (float*)(smem + L_REDA) + (w * 4 + g) * 16 + (c & 3);
#pragma unroll
        for (int r = 0; r < 4; ++r) rp[4 * r] = d[r];
      }
    }
  };
  auto q_publish = [&](unsigned tag) {
    const f2* rp = (const f2*)(smem + L_REDA) + (lane & 7);
    f2 a[12];
#pragma unroll
    for (int i = 0; i < 12; ++i) a[i] = rp[8 * i];
    const f2 t = (((a[0] + a[1]) + (a[2] + a[3])) + ((a[4] + a[5]) + (a[6] + a[7]))) + ((a[8] + a[9]) + (a[10] + a[11]));
    v2u gr = {pk_bf(t[0], t[1]), tag};
    __builtin_amdgcn_raw_buffer_store_b64(gr, rsW, lane < 8 ? WS_QG + (qs * 8 + lane) * 8 : OOB, 0, AUX_SC1);
  };
  auto q_keep = [&](const v4u& v) { v2u o = {v[0], v[2]}; *(v2u*)(smem + L_QB + lane * 8) = o; };

  unsigned long long pc;
  unsigned code_len;
  asm volatile("s_getpc_b64 %0\n.Lfd_code_from:\n\ts_mov_b32 %1, .Lfd_code_to-.Lfd_code_from" : "=s"(pc), "=s"(code_len));
  auto warm_code = [&]() { touch(lbase + L_PAD, lane * 64, mk_rsrc((const void*)pc, code_len), (b >> 3) * 4096); };
  auto put_tb = [&](unsigned tag, unsigned t) {
    if (b >= NWG - 8 && lane == 0) { v2u tb = {t, tag}; __builtin_amdgcn_raw_buffer_store_b64(tb, rsW, WS_TB + (b & 7) * 8, 0, AUX_SC1); }
  };
  auto keep_tb = [&](const v4u& tb) {
    int d = max((int)(tb[0] - t_go), (int)(tb[2] - t_go));
    d = max(d, __builtin_amdgcn_mov_dpp(d, 0xB1, 0xf, 0xf, true));
    d = max(d, __builtin_amdgcn_mov_dpp(d, 0x4E, 0xf, 0xf, true));
    lds_put(lbase + L_FLAG + 28, t_go + d);
  };

  unsigned tagbase;
  auto TAG = [&](unsigned ph) { return 0x80000000u | ((tagbase + ph) & 0x7fffffffu); };
  auto lds_wait = [&](int word) { for (unsigned i = 0; i < 8u * SPIN_CAP && lds_get(lbase + L_FLAG + 4 * word) == 0u; ++i) __builtin_amdgcn_s_sleep(1); };
  auto head_step = [&](auto Qi) {
    constexpr int q = Qi.value;
    if constexpr (q == D) { if (w != 3) { wait_vm<4 * (D - 1) + 12>(); t_go = now8(); } due = t_go + PACE_OFF1 * 256; }
    if constexpr (q >= D) wait_vm<4 * (D - 1) + (q - D < D ? 12 : 0)>();
    gate(PACE_R);
    if constexpr (q < NR) send_k(q, q);
    else if constexpr (q < 2 * NR) { issue_v<vr_in_a(q - NR) ? 0 : 1>(VR[q - NR], voffV, rsV, toff(q - NR)); read_k<q - NR>(KR[q - NR], addrK); }
    else send_k(q - 2 * NR, q - NR);
  };
  {
    v2u px[3];
    unsigned ep;
    send_k(0, 0);
    __builtin_amdgcn_sched_barrier(0);
    {
      const v4u z = {0u, 0u, 0u, 0u};
      if (tid < 144) __builtin_amdgcn_raw_buffer_store_b128(z, rsW, WS_PA + (head * 16 + tid / 9) * PA_STRIDE + split * PA_MSG + (tid % 9) * 16, 0, AUX_SC1);
      __builtin_amdgcn_raw_buffer_store_b128(z, rsW, WS_PB + (tid >> 3) * PB_STRIDE + b * 128 + (tid & 7) * 16, 0, AUX_SC1);
      if (tid < 32) __builtin_amdgcn_raw_buffer_store_b128(z, rsW, WS_PB + (32 + (tid >> 3)) * PB_STRIDE + b * 128 + (tid & 7) * 16, 0, AUX_SC1);
      if (myr >= 0 && tid < 4 * XREP) __builtin_amdgcn_raw_buffer_store_b128(z, rsW, WS_XG + (tid >> 2) * XG_STRIDE + myr * 64 + (tid & 3) * 16, 0, AUX_SC1);
      asm volatile("" ::: "memory");
    }
    const v4i rsX = mk_rsrc(xin, DM * 2);
    const v4i rsE = mk_rsrc(ws, 64);
    asm volatile("buffer_load_dwordx2 %0, %4, %7, 0 offen\n\t"
                 "buffer_load_dwordx2 %1, %5, %7, 0 offen\n\t"
                 "buffer_load_dwordx2 %2, %6, %7, 0 offen\n\t"
                 "buffer_load_dword %3, off, %8, 0 sc1"
                 : "=&v"(px[0]), "=&v"(px[1]), "=&v"(px[2]), "=&v"(ep)
                 : "v"(xpiece(48, 0) * 8), "v"(xpiece(48, 1) * 8), "v"(lane < 16 ? xpiece(48, 2) * 8 : OOB), "s"(rsX), "s"(rsE) : "memory");
    sfor<1, D>([&](auto I) { send_k(I.value, I.value); });
    issue_wq();
    if (w == 2) warm_code();
    if (tid < 8) *(unsigned*)(smem + L_FLAG + 4 * tid) = 0u;
    wg_barrier();
    if (w == 3) {
      asm volatile("s_waitcnt vmcnt(%4)" : "+v"(px[0]), "+v"(px[1]), "+v"(px[2]), "+v"(ep) : "i"(4 * (D - 1) + 12) : "memory");
      tagbase = __builtin_amdgcn_readfirstlane(ep);
      t_go = now8();
      put_tb(TAG(12), t_go);
#pragma unroll
      for (int k = 0; k < 3; ++k) if (((lane + 64 * k) >> 2) == myr) *(v2u*)(smem + L_XR + (lane & 3) * 8) = px[k];
    }
    sfor<D, QG_AT>(head_step);
    wait_wq<4 * (QG_AT - D)>(wqf);
    asm volatile("" : "+v"(ep), "+v"(px[0]), "+v"(px[1]), "+v"(px[2]));
    tagbase = __builtin_amdgcn_readfirstlane(ep);
    if (w < 3) layernorm(px, 48);
  }
  if (w < 3) {
    q_gemv();
    lds_put(lbase + L_FLAG + 4 + 4 * w, 1u);
  }
  v4u ride;
  def_v(ride);
  if (w == 3) ride_load(ride, rsC, WS_TB + (lane & 3) * 16);
  sfor<QG_AT, QG_AT + D + 1>(head_step);
  if (w == 3) {
    v4u a[3];
    a[0] = ride_take(ride);
    if (untagged(a[0], TAG(12)) != 0ull) poll3(a, rsC, WS_TB + (lane & 3) * 16, WS_TB + (lane & 3) * 16, OOB, TAG(12), 0ull);
    keep_tb(a[0]);
    lds_put(lbase + L_FLAG + 20, 1u);
    lds_wait(1); lds_wait(2); lds_wait(3);
    q_publish(TAG(1));
  }
  lds_wait(5);
  due += lds_get(lbase + L_FLAG + 28) + TB_DELTA * 256 - t_go;
  sfor<QG_AT + D + 1, 2 * NR - 2>(head_step);
  if (w == 3) ride_load(ride, rsC, WS_QG + head * 1024 + lane * 16);
  sfor<2 * NR - 2, 2 * NR + D>(head_step);
  if (w == 3) {
    v4u a[3];
    a[0] = ride_take(ride);
    if (untagged(a[0], TAG(1)) != 0ull) poll3(a, rsC, WS_QG + head * 1024 + lane * 16, WS_QG + head * 1024 + lane * 16, OOB, TAG(1), 0ull);
    q_keep(a[0]);
    lds_put(lbase + L_FLAG + 16, 1u);
  }
  lds_wait(4);

  Tile Q;
  Soft st, st2;
  unsigned moved;
  auto load_q = [&]() {
    const v4u* qp = (const v4u*)(smem + L_QB + (c >> 3) * 256 + g * 64);
#pragma unroll
    for (int j = 0; j < 4; ++j) Q.r[j] = qp[j];
    st.acc = (f4){0.f, 0.f, 0.f, 0.f};
    st.mc = -__builtin_inff();
    st.l = 0.f;
    st2 = st;
  };
  auto merge_st2 = [&]() {
    const float m = fmaxf(st.mc, st2.mc), a1 = ex2(st.mc - m), a2 = ex2(st2.mc - m);
    st.acc = st.acc * a1 + st2.acc * a2;
    st.l = st.l * a1 + st2.l * a2;
    st.mc = m;
  };
  auto tile_r = [&](Soft& s, auto I) { pv_tile<vr_in_a(I.value)>(s, softmax_tile<false>(s, qk(KR[I.value], Q), nullptr, moved, 0u), VR[I.value], mk); };
  auto tile_l = [&](Soft& s, auto I) {
    Tile kt;
    read_k<I.value>(kt, addrK);
    pv_tile<true>(s, softmax_tile<false>(s, qk(kt, Q), nullptr, moved, 0u), VL[I.value], mk);
  };
  WoFrag wo1, wo2;
  auto load_wo = [&](WoFrag& x) {
    const unsigned char* p = wo + (size_t)(b * 16 + 4 * g) * (DM * 2);
#pragma unroll
    for (int n = 0; n < 4; ++n) x.f[n] = ld_w<true>(p + n * (DM * 2) + 256 * w + 16 * c);
#pragma unroll
    for (int n = 0; n < 2; ++n) x.c[n] = ld_w<true>(p + (2 * n + (c >> 3)) * (DM * 2) + 1024 + 16 * (c & 7));
    asm volatile("" ::: "memory");
  };
  auto block = [&](auto NTc, auto PARKc, float* alp, unsigned t0, unsigned pace, bool more, auto&& side) {
    constexpr int NT = NTc.value;
    constexpr bool PARK = PARKc.value;
    v2u pend[NT];
    moved = 0u;
    sfor<0, 2 * NT>([&](auto Sc) {
      constexpr int s = Sc.value, q = s + D;
      if constexpr (s + D - 1 < 2 * NT) wait_vm<4 * (D - 1)>();
      else { if (more) wait_vm<4 * (D - 1)>(); else wait_vm<4 * (2 * NT - 1 - s)>(); }
      if constexpr (s < NT) {
        Tile kt;
        read_k<s % D>(kt, addrK);
        if constexpr (q < NT) { gate(pace); send_k(q % D, t0 + q); }
        else { gate(pace); issue_v<2>(VL[(q - NT) % D], voffV, rsV, toff(t0 + q - NT)); }
        const v2u pp = softmax_tile<true>(st, qk(kt, Q), alp + s * 64, moved, 1u << s);
        pend[s][0] = park<PARK>(pp[0]); pend[s][1] = park<PARK>(pp[1]);
      } else {
        constexpr int j = s - NT;
        if (moved & (1u << j)) st.acc *= alp[j * 64];
        const v2u pp = {unpark<PARK>(pend[j][0]), unpark<PARK>(pend[j][1])};
        pv_tile<true>(st, pp, VL[j % D], mk);
        if constexpr (q < 2 * NT) { gate(pace); issue_v<2>(VL[(q - NT) % D], voffV, rsV, toff(t0 + q - NT)); }
        else if (more) { gate(pace); send_k(q - 2 * NT, t0 + NT + q - 2 * NT); }
      }
      side(Sc);
    });
  };
  using I0 = std::integral_constant<int, 0>;
  using I1 = std::integral_constant<int, 1>;

  auto warm = [&](int wi) {
    const v4i rsG = mk_rsrc(wg, DM * DFF * 2), rsU = mk_rsrc(wu, DM * DFF * 2), rsD = mk_rsrc(wd, DM * DFF * 2);
    const unsigned q = (b >> 3) * 1536u + tid + 256 * wi, r = q / 72u;
    const unsigned vg = r * (DFF * 2) + (q - r * 72u) * 16u;
    touch16(lbase + L_PAD, vg, rsG, (b & 7) * 1152u);
    touch16(lbase + L_PAD, vg, rsU, (b & 7) * 1152u);
    touch16(lbase + L_PAD, q < 41472u ? q * 16u : OOB, rsD, (b & 7) * (576u * DM * 2));
  };
  auto chain = [&](auto LAYER, const WoFrag& wf) {
    constexpr int layer = LAYER.value;
    relane();
    const unsigned ph = 2 + 5 * layer;

    const int mu = (b & 7) * 18 + (b >> 3);
    constexpr int GW = layer == 0 ? GATE_WIN : 27;
    v4u wb[GW];
    const unsigned char* pgu;
    unsigned odn, odc;
    auto bases = [&]() {
      pgu = (((c >> 2) & 1) ? wu : wg) + (size_t)(144 * w + (2 * g + (c >> 3)) * 18) * (DFF * 2) + 64 * mu + 16 * (c & 3);
      odn = (32 * mu + 8 * w + 2 * g) * (DM * 2) + 16 * c;
      odc = (32 * mu + 8 * w + 2 * g + (c >> 3)) * (DM * 2) + 1024 + 16 * (c & 7);
    };
    auto frag = [&](auto F) {
      constexpr int f = F.value;
      if constexpr (f < 18) wb[f % GW] = ld_w(pgu + (size_t)f * (DFF * 2));
      else if constexpr (f < 26) wb[f % GW] = ld_w(wd + odn + ((f - 18) & 1) * (DM * 2) + ((f - 18) >> 1) * 256);
      else if constexpr (f == 26) wb[f % GW] = ld_w(wd + odc);
    };
    {
      const float lt = xrow_sum(st.l);
      float* cp = (float*)(smem + L_REDB) + w * (NTA * 64);
#pragma unroll
      for (int r = 0; r < 4; ++r) cp[(c >> 3) * 128 + 32 * g + 8 * r + (c & 7)] = st.acc[r];
      if ((lane & 0x37) == 0) { f2 ml = {st.mc, lt}; *(f2*)(cp + 256 + 2 * (c >> 3)) = ml; }
    }
    wg_barrier();
    {
      const float* cp = (const float*)(smem + L_REDB);
      const int h = tid >> 7;
      f2 ml[4];
#pragma unroll
      for (int i = 0; i < 4; ++i) ml[i] = *(const f2*)(cp + i * (NTA * 64) + 256 + 2 * h);
      const float M = fmaxf(fmaxf(ml[0][0], ml[1][0]), fmaxf(ml[2][0], ml[3][0]));
      float O = 0.f, L = 0.f;
#pragma unroll
      for (int i = 0; i < 4; ++i) { const float sc = ex2(ml[i][0] - M); O += sc * cp[i * (NTA * 64) + tid]; L += sc * ml[i][1]; }
      const unsigned tg = TAG(ph);
      const unsigned pa = WS_PA + (head * 16 + (tid >> 4)) * PA_STRIDE + split * PA_MSG;
      v2u gr = {__float_as_uint(O), tg};
      __builtin_amdgcn_raw_buffer_store_b64(gr, rsW, pa + c * 8, 0, AUX_SC1);
      if (c == 0) {
        v4u g2 = {__float_as_uint(M), tg, __float_as_uint(L), tg};
        __builtin_amdgcn_raw_buffer_store_b128(g2, rsW, pa + 128, 0, AUX_SC1);
      }
    }
    relane();
    {
      const unsigned tg = TAG(ph);
      const unsigned base = WS_PA + b * PA_STRIDE + c * PA_MSG;
      v4u a[3];
      const unsigned tb = TAG(ph + 1);
      const bool hp = (c >> 3) != 0, odd = (c & 1) != 0;
      const unsigned lo_ = WS_PB + b * 128 + (odd ? 64 : 0) + (c & 6) * 8;
      unsigned ob1 = lo_ + (8 * w + 2 * g + (hp ? 1 : 0)) * PB_STRIDE, ob2 = g == (w >> 1) && !hp ? lo_ + (32 + w) * PB_STRIDE : OOB;
      pin(mk); pin(ob1); pin(ob2);
      __builtin_amdgcn_s_sleep(6);
      poll3(a, rsC, base + 32 * g, base + 32 * g + 16, base + 128, tg, ~0ull);
      const float ms = __uint_as_float(a[2][0]);
      const float wgt = ex2(ms - row_max(ms));
      float o[5] = {wgt * __uint_as_float(a[0][0]), wgt * __uint_as_float(a[0][2]), wgt * __uint_as_float(a[1][0]), wgt * __uint_as_float(a[1][2]),
                    wgt * __uint_as_float(a[2][2])};
      row_sums(o);
      const float inv = frcp(o[4]);
#pragma unroll
      for (int n = 0; n < 4; ++n) o[n] *= inv;
      auto put_line = [&](float lo, float hi, unsigned off) {
        const float lon = dpp<0xB1>(lo), hin = dpp<0xB1>(hi);
        v4u gr = {__float_as_uint(odd ? hin : lo), tb, __float_as_uint(odd ? hi : lon), tb};
        __builtin_amdgcn_raw_buffer_store_b128(gr, rsW, off, 0, AUX_SC1);
      };
      const float oa = hp ? o[1] : o[0], obv = hp ? o[3] : o[2];
      f4 e = mfma_first(wf.c[0], diag(pk_bf(oa, oa), mk));
      mfma_acc(e, wf.c[1], diag(pk_bf(obv, obv), mk));
      f4 d = mfma_first(wf.f[0], diag(pk_bf(o[0], o[0]), mk));
#pragma unroll
      for (int n = 1; n < 4; ++n) mfma_acc(d, wf.f[n], diag(pk_bf(o[n], o[n]), mk));
      mfma_fence2(d, e);
      put_line(hp ? d[2] : d[0], hp ? d[3] : d[1], ob1);
      {
        const float lo = (w & 1) ? e[2] : e[0], hi = (w & 1) ? e[3] : e[1];
        float f[2] = {(g >> 1) == (hp ? 1 : 0) ? lo : 0.f, (g >> 1) == (hp ? 1 : 0) ? hi : 0.f};
#pragma unroll
        for (int r = 0; r < 2; ++r) {
          const float t = f[r] + dpp<0x128>(f[r]);
          auto sw = __builtin_amdgcn_permlane32_swap(__float_as_uint(t), __float_as_uint(t), false, false);
          f[r] = __uint_as_float(sw[0]) + __uint_as_float(sw[1]);
        }
        put_line(f[0], f[1], ob2);
      }
    }
    relane();
    if (layer == 0 && myr < 0) {
#pragma unroll
      for (int i = WARM1; i < 6; ++i) warm(i);
      if (w == 2) warm_code();
    }
    auto reduce_cols = [&](auto NPc, unsigned tg, auto FINAL, unsigned tgx) {
      constexpr int NP = NPc.value, NK = (NP * 8 + 255) / 256;
      v4u a[8];
      unsigned long long miss[8];
#pragma unroll
      for (int k = 0; k < 8; ++k) { a[k] = (v4u){0u, 0u, 0u, 0u}; miss[k] = __builtin_amdgcn_ballot_w64(tid + 256 * k < NP * 8); }
      const unsigned base = WS_PB + myr * PB_STRIDE;
      v2u xo = *(const v2u*)(smem + L_XR + (tid & 3) * 8);
      unsigned ox = FINAL.value ? (tid < 4 ? 32 * myr + 8 * tid : OOB) : (tid < 4 * XREP ? WS_XG + (tid >> 2) * XG_STRIDE + myr * 64 + (tid & 3) * 16 : OOB);
      unsigned x0 = xo[0], x1 = xo[1], ra = L_REDA + (tid & 3) * 16;
      pin(x0); pin(x1); pin(ox); pin(ra);
      if constexpr (NP == NWG) __builtin_amdgcn_s_sleep(12);
#pragma unroll 1
      for (unsigned spins = 0; spins < SPIN_CAP; ++spins) {
        ldm4(a[0], a[1], a[2], a[3], rsC, tid * 16, base, miss[0], miss[1], miss[2], miss[3]);
        if ((miss[4] | miss[5] | miss[6] | miss[7]) != 0ull) ldm4(a[4], a[5], a[6], a[7], rsC, tid * 16, base + 16384, miss[4], miss[5], miss[6], miss[7]);
        pin4(a[4], a[5], a[6], a[7]);
        pin4(a[0], a[1], a[2], a[3]);
        unsigned long long any = 0ull;
#pragma unroll
        for (int k = 0; k < NK; ++k) { miss[k] &= untagged(a[k], tg); any |= miss[k]; }
        if (any == 0ull) break;
      }
      float s0 = __uint_as_float(a[0][0]), s1 = __uint_as_float(a[0][2]);
#pragma unroll
      for (int k = 1; k < NK; ++k) { s0 += __uint_as_float(a[k][0]); s1 += __uint_as_float(a[k][2]); }
      s0 += dpp<0x128>(s0); s1 += dpp<0x128>(s1);
      xrow_sums2(s0, s1);
      if (lane < 8) ((f2*)(smem + L_REDA))[w * 8 + lane] = (f2){s0, s1};
      wg_barrier();
      if (w == 0) {
        const f4* rp = (const f4*)(smem + ra);
        const f4 t = (rp[0] + rp[4]) + (rp[8] + rp[12]);
        const unsigned t0 = pk_bf(t[0], t[1]), t1 = pk_bf(t[2], t[3]);
        const v2u xn = {pk_bf(bf_lo(x0) + bf_lo(t0), bf_hi(x0) + bf_hi(t0)), pk_bf(bf_lo(x1) + bf_lo(t1), bf_hi(x1) + bf_hi(t1))};
        if constexpr (FINAL.value) {
          __builtin_amdgcn_raw_buffer_store_b64(xn, __builtin_amdgcn_make_buffer_rsrc(out, 0, DM * 2, 0x00020000), ox, 0, 0);
        } else {
          const v4u gx = {xn[0], tgx, xn[1], tgx};
          __builtin_amdgcn_raw_buffer_store_b128(gx, rsW, ox, 0, AUX_SC1);
          if (tid < 4) *(v2u*)(smem + L_XR + tid * 8) = xn;
        }
      }
    };
    auto gather_x_ln = [&](unsigned tg, int np) {
      v4u a[3];
      const unsigned xb = WS_XG + (b % XREP) * XG_STRIDE;
      poll3(a, rsC, xb + xpiece(np, 0) * 16, xb + xpiece(np, 1) * 16, lane < 16 ? xb + xpiece(np, 2) * 16 : OOB, tg, 0xffffull);
      v2u px[3];
#pragma unroll
      for (int k = 0; k < 3; ++k) { px[k][0] = a[k][0]; px[k][1] = a[k][2]; }
      layernorm(px, np);
    };

    if (b < NMLP) {
      bases();
      if constexpr (layer == 0) wait_vm<0>();
      sfor<0, GW>(frag);
      unsigned xoff = L_XB + (144 * w + (2 * g + (c >> 3)) * 18) * 2;
      if constexpr (layer != 0) { pin(mk); pin(xoff); }
      gather_x_ln(TAG(ph + 2), 36);
      if constexpr (layer == 0) { relane(); bases(); xoff = L_XB + (144 * w + (2 * g + (c >> 3)) * 18) * 2; }
      f4 d;
      {
        const unsigned* xp = (const unsigned*)(smem + xoff);
        unsigned xs[9];
        constexpr int XA = layer == 0 ? 4 : 9;
#pragma unroll
        for (int n = 0; n < XA; ++n) xs[n] = xp[n];
        sfor<0, 9>([&](auto Nn) {
          constexpr int n = Nn.value;
          const v4u b0 = diag(dup_lo(xs[n]), mk), b1 = diag(dup_hi(xs[n]), mk);
          if constexpr (n + XA < 9) xs[n + XA] = xp[n + XA];
          if constexpr (n == 0) d = mfma_first2(wb[0], b0, wb[1], b1); else mfma_acc2(d, wb[(2 * n) % GW], b0, wb[(2 * n + 1) % GW], b1);
          if constexpr (layer == 0) __builtin_amdgcn_sched_barrier(0);
          frag(std::integral_constant<int, 2 * n + GW>{});
          frag(std::integral_constant<int, 2 * n + 1 + GW>{});
          if constexpr (layer == 0) __builtin_amdgcn_sched_barrier(0);
        });
      }
      const bool hp = (c >> 3) != 0;
      unsigned ra = L_REDA + ((w * 4 + g) * 32 + (c & 7)) * 4, rb = L_REDA + (4 * w + g + 32 * (c & 7) + 16 * (c >> 3)) * 8;
      unsigned rc_ = L_REDB + (w * 512 + 32 * g + (c & 7)) * 4, rd = L_REDB + (2048 + w * 128 + 32 * g + (c & 7)) * 4;
      unsigned o1 = WS_PB + (tid >> 3) * PB_STRIDE + mu * 128 + (tid & 7) * 16, o2 = lane < 8 ? WS_PB + (32 + w) * PB_STRIDE + mu * 128 + lane * 16 : OOB;
      pin(ra); pin(rb); pin(rc_); pin(rd); pin(o1); pin(o2);
      mfma_fence(d);
      if ((g >> 1) == (c >> 3)) {
        float* rp = (float*)(smem + ra);
#pragma unroll
        for (int r = 0; r < 4; ++r) rp[8 * r] = d[r];
      }
      wg_barrier();
      unsigned hd0, hd1;
      {
        const f2 v = *(const f2*)(smem + rb);
        float t[2] = {v[0], v[1]};
#pragma unroll
        for (int i = 0; i < 2; ++i) t[i] += dpp<0xB1>(t[i]);
#pragma unroll
        for (int i = 0; i < 2; ++i) t[i] += dpp<0x4E>(t[i]);
#pragma unroll
        for (int i = 0; i < 2; ++i) t[i] += dpp<0x141>(t[i]);
        const float y = dpp<0x128>(hp ? t[0] : t[1]), own = hp ? t[1] : t[0];
        const float x = rbf(hp ? y : own), up = rbf(hp ? own : y);
        const float z = 0.7978845608028654f * (x + 0.044715f * x * x * x);
        const float th = 1.f - 2.f * __builtin_amdgcn_rcpf(__expf(2.f * z) + 1.f);
        const float hv = rbf(0.5f * x * (1.f + th)) * up;
        const unsigned hd = pk_bf(hv, hv), ho = __builtin_amdgcn_mov_dpp(hd, 0x128, 0xf, 0xf, true);
        hd0 = hp ? ho : hd; hd1 = hp ? hd : ho;
        f4 e[5];
        const v4u b0 = diag(hd0, mk), b1 = diag(hd1, mk);
#pragma unroll
        for (int bl = 0; bl < 4; ++bl) e[bl] = mfma_first2(wb[(18 + 2 * bl) % GW], b0, wb[(19 + 2 * bl) % GW], b1);
        e[4] = mfma_first(wb[26 % GW], diag(hd, mk));
        mfma_fence5(e);
        if (!hp) {
#pragma unroll
          for (int bl = 0; bl < 4; ++bl) {
            float* q = (float*)(smem + rc_) + 128 * bl;
#pragma unroll
            for (int r = 0; r < 4; ++r) q[8 * r] = e[bl][r];
          }
        }
        if ((g >> 1) == (c >> 3)) {
          float* q = (float*)(smem + rd);
#pragma unroll
          for (int r = 0; r < 4; ++r) q[8 * r] = e[4][r];
        }
      }
      wg_barrier();
      {
        const f2* rp = (const f2*)(smem + L_REDB);
        const unsigned tg = TAG(ph + 3);
        f2 m[4], x[8];
#pragma unroll
        for (int i = 0; i < 4; ++i) m[i] = rp[256 * i + tid];
#pragma unroll
        for (int i = 0; i < 8; ++i) x[i] = rp[1024 + 32 * i + 8 * w + (lane & 7)];
        const f2 a = (m[0] + m[1]) + (m[2] + m[3]);
        v4u g0 = {__float_as_uint(a[0]), tg, __float_as_uint(a[1]), tg};
        __builtin_amdgcn_raw_buffer_store_b128(g0, rsW, o1, 0, AUX_SC1);
        const f2 e = ((x[0] + x[1]) + (x[2] + x[3])) + ((x[4] + x[5]) + (x[6] + x[7]));
        v4u g1 = {__float_as_uint(e[0]), tg, __float_as_uint(e[1]), tg};
        __builtin_amdgcn_raw_buffer_store_b128(g1, rsW, o2, 0, AUX_SC1);
      }
      if constexpr (layer == 0) issue_wq();
    } else {
      if (myr >= 0) {
        reduce_cols(std::integral_constant<int, NWG>{}, TAG(ph + 1), std::false_type{}, TAG(ph + 2));
      }
      if constexpr (layer == 0) issue_wq();
      if (myr >= 0) {
        reduce_cols(std::integral_constant<int, NMLP>{}, TAG(ph + 3), std::integral_constant<bool, layer == 1>{}, TAG(ph + 4));
        if (layer == 1 && myr == 0 && tid == 0) __builtin_amdgcn_raw_buffer_store_b32(tagbase + 16u, rsW, WS_EPOCH, 0, AUX_SC1);
      }
    }
    if constexpr (layer == 0) {
      relane();
      pin(mk);
      if (w < 3) gather_x_ln(TAG(ph + 4), 48);
      if (w == 0) put_tb(TAG(ph + 5), now8());
      wait_wq<0>(wqf);
      q_gemv();
      wg_barrier();
      if (w == 3) {
        q_publish(TAG(ph + 5));
        __builtin_amdgcn_s_sleep(6);
        v4u a[3];
        poll3(a, rsC, WS_QG + head * 1024 + lane * 16, WS_TB + (lane & 3) * 16, OOB, TAG(ph + 5), 0ull);
        q_keep(a[0]);
        keep_tb(a[1]);
      }
      wg_barrier();
    }
  };

  load_q();
  sfor<0, D>([&](auto I) { def_a(VL[I.value]); });
  float* alp_ks = (float*)(smem + L_KS + w * (NL * 4096) + D * 4096) + lane;
#pragma unroll 1
  for (int i = 0; i < NB1; ++i) block(std::integral_constant<int, NT1>{}, std::true_type{}, alp_ks, NR + i * NT1, PACE_B1, true, [](auto) {});
  sfor<D, 2 * NL>([&](auto Qi) {
    constexpr int q = Qi.value;
    wait_vm<4 * (D - 1)>();
    if constexpr (q - D >= NL) tile_l(st, std::integral_constant<int, q - D - NL>{});
    gate(PACE_B1);
    if constexpr (q < NL) send_k(q, NTILE - NL + q);
    else issue_v<(q - NL < D) ? 2 : 0>(VL[q - NL], voffV, rsV, toff(NTILE - 2 * NL + q));
    if constexpr (q - D < NR) tile_r(st, std::integral_constant<int, q - D>{});
  });
#pragma unroll
  for (int i = 0; i < WARM1; ++i) warm(i);
  sfor<NL - D, NL>([&](auto I) {
    wait_vm<4 * (NL - 1 - I.value) + 3 * WARM1 + (I.value == NL - 1 ? 6 : 0)>();
    tile_l(st, I);
    if constexpr (I.value == NL - 2) load_wo(wo1);
  });
  chain(I0{}, wo1);
  relane();
  due = lds_get(lbase + L_FLAG + 28) + PACE_OFF2 * 256;
  load_q();
  sfor<0, D>([&](auto I) {
    tile_l(st2, I);
    if constexpr (I.value > 0) gate(PACE_A2);
    send_k(I.value, NR + I.value);
  });
  block(std::integral_constant<int, NTA>{}, std::false_type{}, (float*)(smem + L_REDB) + w * (NTA * 64) + lane, NR, PACE_A2, true, [&](auto S) {
    constexpr int nk = NR + NL - D, ink = nk - NTA, s = S.value;
    constexpr int k = s >= NTA ? ink + (s - NTA) : ((s * ink) / NTA != ((s + 1) * ink) / NTA ? (s * ink) / NTA : -1);
    if constexpr (k >= NR) tile_l(st2, std::integral_constant<int, k - NR + D>{});
    else if constexpr (k >= 0) tile_r(st2, std::integral_constant<int, k>{});
  });
  merge_st2();
  due += SLACK2 * 256;
  alp_ks = (float*)(smem + L_KS + w * (NL * 4096) + D * 4096) + lane;
#pragma unroll 1
  for (int i = 0; i < NB2; ++i) block(std::integral_constant<int, NT2>{}, std::false_type{}, alp_ks, NR + NTA + i * NT2, PACE_B2, i < NB2 - 1, [](auto) {});
  load_wo(wo2);
  chain(I1{}, wo2);
  asm volatile(".Lfd_code_to:");
}

}

extern "C" void kernel_launch(void* const* d_in, const int* in_sizes, int n_in, void* d_out, int out_size, void* d_ws, size_t ws_size,
                              hipStream_t stream) {
  (void)in_sizes; (void)n_in; (void)out_size; (void)ws_size;
  fused_decode_kernel<<<NWG, 256, 0, stream>>>(
      (const unsigned short*)d_in[0], (const unsigned char*)d_in[1], (const unsigned char*)d_in[2], (const unsigned char*)d_in[3],
      (const unsigned char*)d_in[4], (const unsigned char*)d_in[6], (const unsigned char*)d_in[7], (const unsigned char*)d_in[8],
      (unsigned short*)d_out, (unsigned char*)d_ws);
}
```

```cpp
#include <hip/hip_runtime.h>
#include <hip/hip_bf16.h>
#include <type_traits>

namespace {

typedef unsigned v4u __attribute__((ext_vector_type(4)));
typedef unsigned v2u __attribute__((ext_vector_type(2)));
typedef int v4i __attribute__((ext_vector_type(4)));
typedef float f4 __attribute__((ext_vector_type(4)));
typedef float f2 __attribute__((ext_vector_type(2)));
typedef __bf16 bf2 __attribute__((ext_vector_type(2)));
#define FI __device__ __forceinline__

constexpr int DM = 576, DFF = 4608, NQ = 4096;
constexpr int NWG = 256;
constexpr int NMLP = 144;
constexpr int NRED = 36;
constexpr int XREP = 8;
constexpr int NTILE = 64;
constexpr int NR = 9;
constexpr int NL = 9;
constexpr int D = 3;
constexpr int NSTREAM = NTILE - NR - NL, NTA = 10, NB1 = 2, NT1 = NSTREAM / NB1, NB2 = 2, NT2 = (NSTREAM - NTA) / NB2;
static_assert(NB1 * NT1 == NSTREAM && NTA + NB2 * NT2 == NSTREAM && D >= 2 && D <= 4 && NR + NL - D <= 2 * NTA && NT1 <= 32 && NT2 <= 32, "schedule");
static_assert(NT1 * 256 <= (NL - D) * 4096 && 2 * NT1 <= (NL - D) * 16, "room for the rescale factors and the parked probabilities");
static_assert(4 * XREP <= 64, "wave 0 writes every copy of x");
constexpr int GATE_WIN = 12;
constexpr int WARM1 = 2;
constexpr int QG_AT = 5;
constexpr unsigned PACE_R = 14464, PACE_B1 = 14080, PACE_A2 = 14208, PACE_B2 = 14144;
constexpr int PACE_OFF1 = -10, TB_DELTA = 40, PACE_OFF2 = 180, SLACK2 = 40;
static_assert(GATE_WIN >= 9, "all down fragments must fit the window");
constexpr float QSCALE = 0.08838834764831845f * 1.4426950408889634f;
constexpr unsigned SPIN_CAP = 1u << 21;

constexpr unsigned WS_EPOCH = 0;
constexpr unsigned WS_TB = 256;
constexpr unsigned WS_QG = 4096;
constexpr unsigned WS_XG = 32768, XG_STRIDE = 2560;
constexpr unsigned WS_PA = 65536 + 32768, PA_MSG = 160, PA_STRIDE = 16 * PA_MSG;
constexpr unsigned WS_PB = 1u << 20, PB_STRIDE = NWG * 128;
constexpr unsigned WS_BYTES = WS_PB + NRED * PB_STRIDE;
constexpr unsigned OOB = 0x7fffff00u;

constexpr int L_KS = 0;
constexpr int L_SC = 4 * NL * 4096;
constexpr int L_REDB = L_SC;
constexpr int L_REDA = L_SC + 10240;
constexpr int L_XB = L_REDA + 2048;
constexpr int L_QB = L_XB + 1280;
constexpr int L_PAD = L_QB + 512;
constexpr int L_FLAG = L_PAD + 1024;
constexpr int L_XR = L_FLAG + 32;
constexpr int L_BYTES = L_XR + 32;
static_assert(L_BYTES <= 163840, "LDS");

template <int B, int E, class F> FI void sfor(F&& f) {
  if constexpr (B < E) { f(std::integral_constant<int, B>{}); sfor<B + 1, E>(f); }
}

FI v4i mk_rsrc(const void* p, unsigned bytes) {
  unsigned long long a = (unsigned long long)p;
  v4i r = {(int)(unsigned)a, (int)((unsigned)(a >> 32) & 0xffffu), (int)bytes, 0x00020000};
  return r;
}
FI float bf_lo(unsigned u) { return __uint_as_float(u << 16); }
FI float bf_hi(unsigned u) { return __uint_as_float(u & 0xffff0000u); }
FI unsigned pk_bf(float a, float b) { f2 v = {a, b}; return __builtin_bit_cast(unsigned, __builtin_convertvector(v, bf2)); }
FI float rbf(float a) { return bf_lo(pk_bf(a, a)); }
FI float frcp(float x) { const float r = __builtin_amdgcn_rcpf(x); return __builtin_fmaf(__builtin_fmaf(-x, r, 1.f), r, r); }
FI float ex2(float x) { return __builtin_amdgcn_exp2f(x); }

template <int CTRL> FI float dpp(float x) {
  return __builtin_bit_cast(float, __builtin_amdgcn_mov_dpp(__builtin_bit_cast(int, x), CTRL, 0xf, 0xf, true));
}
FI float row_sum(float x) {
  x += dpp<0xB1>(x); x += dpp<0x4E>(x); x += dpp<0x141>(x); x += dpp<0x128>(x); return x;
}
FI float row_max(float x) {
  x = fmaxf(x, dpp<0xB1>(x)); x = fmaxf(x, dpp<0x4E>(x)); x = fmaxf(x, dpp<0x141>(x)); x = fmaxf(x, dpp<0x128>(x)); return x;
}
FI float xrow_max(float x) {
  auto s = __builtin_amdgcn_permlane16_swap(__float_as_uint(x), __float_as_uint(x), false, false);
  x = fmaxf(__uint_as_float(s[0]), __uint_as_float(s[1]));
  auto t = __builtin_amdgcn_permlane32_swap(__float_as_uint(x), __float_as_uint(x), false, false);
  return fmaxf(__uint_as_float(t[0]), __uint_as_float(t[1]));
}
FI float xrow_sum(float x) {
  auto s = __builtin_amdgcn_permlane16_swap(__float_as_uint(x), __float_as_uint(x), false, false);
  x = __uint_as_float(s[0]) + __uint_as_float(s[1]);
  auto t = __builtin_amdgcn_permlane32_swap(__float_as_uint(x), __float_as_uint(x), false, false);
  return __uint_as_float(t[0]) + __uint_as_float(t[1]);
}
FI float wave_sum(float x) { return xrow_sum(row_sum(x)); }
template <int N> FI void row_sums(float (&x)[N]) {
#pragma unroll
  for (int i = 0; i < N; ++i) x[i] += dpp<0xB1>(x[i]);
  __builtin_amdgcn_sched_barrier(0);
#pragma unroll
  for (int i = 0; i < N; ++i) x[i] += dpp<0x4E>(x[i]);
  __builtin_amdgcn_sched_barrier(0);
#pragma unroll
  for (int i = 0; i < N; ++i) x[i] += dpp<0x141>(x[i]);
  __builtin_amdgcn_sched_barrier(0);
#pragma unroll
  for (int i = 0; i < N; ++i) x[i] += dpp<0x128>(x[i]);
  __builtin_amdgcn_sched_barrier(0);
}
FI void xrow_sums2(float& a, float& b) {
  auto s = __builtin_amdgcn_permlane16_swap(__float_as_uint(a), __float_as_uint(b), false, false);
  const float t = __uint_as_float(s[0]) + __uint_as_float(s[1]);
  auto u = __builtin_amdgcn_permlane32_swap(__float_as_uint(t), __float_as_uint(t), false, false);
  const float v = __uint_as_float(u[0]) + __uint_as_float(u[1]);
  auto r = __builtin_amdgcn_permlane16_swap(__float_as_uint(v), __float_as_uint(v), false, false);
  a = __uint_as_float(r[0]); b = __uint_as_float(r[1]);
}
FI void pin(unsigned& a) { asm volatile("" : "+v"(a)); }

FI void wg_barrier() { asm volatile("s_waitcnt lgkmcnt(0)\n\ts_barrier" ::: "memory"); }
template <int N> FI void wait_vm() { asm volatile("s_waitcnt vmcnt(%0)" ::"i"(N > 63 ? 63 : N) : "memory"); }

FI void mfma_acc(f4& d, const v4u& a, const v4u& b) {
  asm("s_nop 1\n\tv_mfma_f32_16x16x32_bf16 %0, %1, %2, %0" : "+v"(d) : "v"(a), "v"(b));
}
FI f4 mfma_first(const v4u& a, const v4u& b) {
  f4 d;
  asm("s_nop 1\n\tv_mfma_f32_16x16x32_bf16 %0, %1, %2, 0" : "=&v"(d) : "v"(a), "v"(b));
  return d;
}
FI void mfma_acc2(f4& d, const v4u& a0, const v4u& b0, const v4u& a1, const v4u& b1) {
  asm("s_nop 1\n\tv_mfma_f32_16x16x32_bf16 %0, %1, %2, %0\n\tv_mfma_f32_16x16x32_bf16 %0, %3, %4, %0" : "+v"(d) : "v"(a0), "v"(b0), "v"(a1), "v"(b1));
}
FI f4 mfma_first2(const v4u& a0, const v4u& b0, const v4u& a1, const v4u& b1) {
  f4 d;
  asm("s_nop 1\n\tv_mfma_f32_16x16x32_bf16 %0, %1, %2, 0\n\tv_mfma_f32_16x16x32_bf16 %0, %3, %4, %0" : "=&v"(d) : "v"(a0), "v"(b0), "v"(a1), "v"(b1));
  return d;
}
FI void mfma_fence(f4& d) { asm("s_nop 7\n\ts_nop 1" : "+v"(d)); }

FI void mfma_fence2(f4& d, f4& e) { asm("s_nop 7\n\ts_nop 1" : "+v"(d), "+v"(e)); }
FI void mfma_fence5(f4 (&e)[5]) { asm("s_nop 7\n\ts_nop 1" : "+v"(e[0]), "+v"(e[1]), "+v"(e[2]), "+v"(e[3]), "+v"(e[4])); }

struct Masks { unsigned m0, m1, m2, m3; };
FI v4u diag(unsigned dup, const Masks& k) { v4u b = {dup & k.m0, dup & k.m1, dup & k.m2, dup & k.m3}; return b; }
FI void pin(Masks& k) { asm volatile("" : "+v"(k.m0), "+v"(k.m1), "+v"(k.m2), "+v"(k.m3)); }
FI unsigned dup_lo(unsigned x) { return __builtin_amdgcn_perm(x, x, 0x01000100u); }
FI unsigned dup_hi(unsigned x) { return __builtin_amdgcn_perm(x, x, 0x03020302u); }

struct Tile { v4u r[4]; };
struct WoFrag { v4u f[4], c[2]; };

struct Ln {
  int lane, g, c;
  Masks mk;
};
FI Ln make_ln() {
  Ln L;
  asm volatile("v_mbcnt_lo_u32_b32 %0, -1, 0\n\tv_mbcnt_hi_u32_b32 %0, -1, %0" : "=v"(L.lane));
  L.g = L.lane >> 4; L.c = L.lane & 15;
  const unsigned hm = (L.c & 1) ? 0xffff0000u : 0x0000ffffu;
  const int e2 = (L.c & 7) >> 1;
  L.mk.m0 = e2 == 0 ? hm : 0u; L.mk.m1 = e2 == 1 ? hm : 0u; L.mk.m2 = e2 == 2 ? hm : 0u; L.mk.m3 = e2 == 3 ? hm : 0u;
  return L;
}

FI void issue_k(unsigned m0v, unsigned voff, const v4i& rs, unsigned soff) {
  unsigned keep, tmp;
  asm volatile(
      "s_nop 1\n\t"
      "s_mov_b32 %0, m0\n\t"
      "s_mov_b32 m0, %2\n\t"
      "v_xor_b32 %1, 64, %3\n\t"
      "buffer_load_dwordx4 %3, %4, %5 offen nt lds\n\t"
      "buffer_load_dwordx4 %1, %4, %5 offen offset:1024 nt lds\n\t"
      "v_xor_b32 %1, 0x80, %3\n\t"
      "buffer_load_dwordx4 %1, %4, %5 offen offset:2048 nt lds\n\t"
      "v_xor_b32 %1, 0xc0, %3\n\t"
      "buffer_load_dwordx4 %1, %4, %5 offen offset:3072 nt lds\n\t"
      "s_mov_b32 m0, %0"
      : "=&s"(keep), "=&v"(tmp)
      : "s"(m0v), "v"(voff), "s"(rs), "s"(soff)
      : "memory");
}
#define V_LOADS \
  "s_nop 4\n\t" \
  "buffer_load_dwordx4 %0, %4, %5, %6 offen nt\n\t" \
  "buffer_load_dwordx4 %1, %4, %5, %6 offen offset:1024 nt\n\t" \
  "buffer_load_dwordx4 %2, %4, %5, %6 offen offset:2048 nt\n\t" \
  "buffer_load_dwordx4 %3, %4, %5, %6 offen offset:3072 nt"
template <int MODE> FI void issue_v(Tile& t, unsigned voff, const v4i& rs, unsigned soff) {
  if constexpr (MODE == 0)
    asm volatile(V_LOADS : "=a"(t.r[0]), "=a"(t.r[1]), "=a"(t.r[2]), "=a"(t.r[3]) : "v"(voff), "s"(rs), "s"(soff) : "memory");
  else if constexpr (MODE == 1)
    asm volatile(V_LOADS : "=&v"(t.r[0]), "=&v"(t.r[1]), "=&v"(t.r[2]), "=&v"(t.r[3]) : "v"(voff), "s"(rs), "s"(soff) : "memory");
  else
    asm volatile(V_LOADS ::"a"(t.r[0]), "a"(t.r[1]), "a"(t.r[2]), "a"(t.r[3]), "v"(voff), "s"(rs), "s"(soff) : "memory");
}
FI void def_a(Tile& t) { asm volatile("" : "=a"(t.r[0]), "=a"(t.r[1]), "=a"(t.r[2]), "=a"(t.r[3])); }
template <bool A> FI unsigned park(unsigned v) {
  if constexpr (A) { unsigned r; asm("v_accvgpr_write_b32 %0, %1" : "=a"(r) : "v"(v)); return r; } else return v;
}
template <bool A> FI unsigned unpark(unsigned r) {
  if constexpr (A) { unsigned v; asm("v_accvgpr_read_b32 %0, %1" : "=v"(v) : "a"(r)); return v; } else return r;
}
FI void touch(unsigned m0v, unsigned voff, const v4i& rs, unsigned soff) {
  unsigned keep;
  asm volatile(
      "s_mov_b32 %0, m0\n\t"
      "s_mov_b32 m0, %1\n\t"
      "s_nop 2\n\t"
      "buffer_load_dword %2, %3, %4 offen lds\n\t"
      "s_mov_b32 m0, %0"
      : "=&s"(keep)
      : "s"(m0v), "v"(voff), "s"(rs), "s"(soff)
      : "memory");
}
FI void touch16(unsigned m0v, unsigned voff, const v4i& rs, unsigned soff) {
  unsigned keep;
  asm volatile(
      "s_mov_b32 %0, m0\n\t"
      "s_mov_b32 m0, %1\n\t"
      "s_nop 2\n\t"
      "buffer_load_dwordx4 %2, %3, %4 offen lds\n\t"
      "s_mov_b32 m0, %0"
      : "=&s"(keep)
      : "s"(m0v), "v"(voff), "s"(rs), "s"(soff)
      : "memory");
}
FI void ride_load(const v4u& q, const v4i& rs, unsigned voff) {
  asm volatile("s_nop 4\n\tbuffer_load_dwordx4 %0, %1, %2, 0 offen sc1" ::"v"(q), "v"(voff), "s"(rs) : "memory");
}
FI v4u ride_take(const v4u& q) {
  v4u r;
  asm volatile("v_mov_b64 %0, %2\n\tv_mov_b64 %1, %3" : "=&v"(*(v2u*)&r), "=&v"(*((v2u*)&r + 1)) : "v"(*(const v2u*)&q), "v"(*((const v2u*)&q + 1)));
  return r;
}
FI void def_v(v4u& t) { asm volatile("" : "=v"(t)); }
FI void ldm3(v4u (&a)[3], const v4i& rs, unsigned v0, unsigned v1, unsigned v2, unsigned long long m) {
  unsigned long long keep;
  asm volatile(
      "s_mov_b64 %3, exec\n\t"
      "s_mov_b64 exec, %8\n\t"
      "s_nop 2\n\t"
      "buffer_load_dwordx4 %0, %5, %4, 0 offen sc1\n\t"
      "buffer_load_dwordx4 %1, %6, %4, 0 offen sc1\n\t"
      "buffer_load_dwordx4 %2, %7, %4, 0 offen sc1\n\t"
      "s_mov_b64 exec, %3\n\t"
      "s_waitcnt vmcnt(0)"
      : "+v"(a[0]), "+v"(a[1]), "+v"(a[2]), "=&s"(keep) : "s"(rs), "v"(v0), "v"(v1), "v"(v2), "s"(m) : "memory");
}
FI unsigned long long untagged(const v4u& a, unsigned tg) {
  return __builtin_amdgcn_ballot_w64(a[1] != tg) | __builtin_amdgcn_ballot_w64(a[3] != tg);
}
FI void poll3(v4u (&a)[3], const v4i& rs, unsigned v0, unsigned v1, unsigned v2, unsigned tg, unsigned long long care2) {
#pragma unroll
  for (int k = 0; k < 3; ++k) a[k] = (v4u){0u, 0u, 0u, 0u};
  unsigned long long miss = ~0ull;
#pragma unroll 1
  for (unsigned spins = 0; spins < SPIN_CAP; ++spins) {
    ldm3(a, rs, v0, v1, v2, miss);
    miss &= untagged(a[0], tg) | untagged(a[1], tg) | (untagged(a[2], tg) & care2);
    if (miss == 0ull) break;
  }
}
#define LDM(i, m) "s_mov_b64 exec, %" #m "\n\tbuffer_load_dwordx4 %" #i ", %7, %6, %5 offen sc1\n\ts_add_u32 %5, %5, 0x1000\n\t"
FI void ldm4(v4u& a0, v4u& a1, v4u& a2, v4u& a3, const v4i& rs, unsigned v0, unsigned so, unsigned long long m0, unsigned long long m1,
             unsigned long long m2, unsigned long long m3) {
  unsigned long long keep;
  asm volatile("s_mov_b64 %4, exec\n\ts_nop 3\n\t" LDM(0, 8) LDM(1, 9) LDM(2, 10) LDM(3, 11) "s_mov_b64 exec, %4"
               : "+v"(a0), "+v"(a1), "+v"(a2), "+v"(a3), "=&s"(keep), "+s"(so)
               : "s"(rs), "v"(v0), "s"(m0), "s"(m1), "s"(m2), "s"(m3)
               : "memory", "scc");
}
FI void pin4(v4u& a0, v4u& a1, v4u& a2, v4u& a3) { asm volatile("s_waitcnt vmcnt(0)" : "+v"(a0), "+v"(a1), "+v"(a2), "+v"(a3) :: "memory"); }
FI void lds_put(unsigned addr, unsigned v) { asm volatile("s_waitcnt lgkmcnt(0)\n\tds_write_b32 %0, %1" ::"v"(addr), "v"(v) : "memory"); }
FI unsigned lds_get(unsigned addr) {
  unsigned v;
  asm volatile("ds_read_b32 %0, %1\n\ts_waitcnt lgkmcnt(0)" : "=v"(v) : "v"(addr) : "memory");
  return __builtin_amdgcn_readfirstlane(v);
}
template <int SLOT> FI void read_k(Tile& k, unsigned addr) {
  unsigned tmp;
  asm volatile(
      "ds_read_b128 %0, %5 offset:%6\n\t"
      "v_xor_b32 %4, 16, %5\n\t"
      "ds_read_b128 %1, %4 offset:%6\n\t"
      "v_xor_b32 %4, 32, %5\n\t"
      "ds_read_b128 %2, %4 offset:%6\n\t"
      "v_xor_b32 %4, 48, %5\n\t"
      "ds_read_b128 %3, %4 offset:%6\n\t"
      "s_waitcnt lgkmcnt(0)"
      : "=&v"(k.r[0]), "=&v"(k.r[1]), "=&v"(k.r[2]), "=&v"(k.r[3]), "=&v"(tmp)
      : "v"(addr), "i"(SLOT * 4096)
      : "memory");
}
FI f4 qk(const Tile& k, const Tile& q) {
  f4 s;
  asm volatile(
      "v_mfma_f32_16x16x32_bf16 %0, %1, %5, 0\n\t"
      "v_mfma_f32_16x16x32_bf16 %0, %2, %6, %0\n\t"
      "v_mfma_f32_16x16x32_bf16 %0, %3, %7, %0\n\t"
      "v_mfma_f32_16x16x32_bf16 %0, %4, %8, %0\n\t"
      "s_nop 7\n\ts_nop 1"
      : "=&v"(s)
      : "v"(k.r[0]), "v"(k.r[1]), "v"(k.r[2]), "v"(k.r[3]), "v"(q.r[0]), "v"(q.r[1]), "v"(q.r[2]), "v"(q.r[3]));
  return s;
}
FI void load_b64(v2u& d, unsigned voff, const v4i& rs, unsigned soff) {
  asm volatile("s_nop 4\n\tbuffer_load_dwordx2 %0, %1, %2, %3 offen sc1" : "=v"(d) : "v"(voff), "s"(rs), "s"(soff) : "memory");
}
template <int N> FI void wait_wq(v2u (&q)[12]) {
  asm volatile("s_waitcnt vmcnt(%12)"
               : "+v"(q[0]), "+v"(q[1]), "+v"(q[2]), "+v"(q[3]), "+v"(q[4]), "+v"(q[5]), "+v"(q[6]), "+v"(q[7]), "+v"(q[8]), "+v"(q[9]),
                 "+v"(q[10]), "+v"(q[11])
               : "i"(N) : "memory");
}

struct Soft { f4 acc; float mc, l; };

template <bool LATER> FI v2u softmax_tile(Soft& st, const f4& s, float* slot, unsigned& moved, unsigned bit) {
  f4 t;
#pragma unroll
  for (int r = 0; r < 4; ++r) t[r] = __builtin_fmaf(s[r], QSCALE, -st.mc);
  const float tm = fmaxf(fmaxf(t[0], t[1]), fmaxf(t[2], t[3]));
  if (__builtin_amdgcn_ballot_w64(tm > 0.f) != 0ull) {
    const float sm = fmaxf(fmaxf(s[0], s[1]), fmaxf(s[2], s[3])) * QSCALE;
    const float mn = fmaxf(st.mc, xrow_max(sm));
    const float al = ex2(st.mc - mn);
    st.mc = mn;
    st.l *= al;
    if constexpr (LATER) { *slot = al; moved |= bit; } else st.acc *= al;
#pragma unroll
    for (int r = 0; r < 4; ++r) t[r] = __builtin_fmaf(s[r], QSCALE, -mn);
  }
  f4 p;
#pragma unroll
  for (int r = 0; r < 4; ++r) p[r] = ex2(t[r]);
  st.l += (p[0] + p[1]) + (p[2] + p[3]);
  v2u pp = {pk_bf(p[0], p[1]), pk_bf(p[2], p[3])};
  return pp;
}
#define PV_MFMAS \
  "s_nop 1\n\t" \
  "v_mfma_f32_16x16x32_bf16 %0, %1, %5, %0\n\t" \
  "v_mfma_f32_16x16x32_bf16 %0, %2, %6, %0\n\t" \
  "v_mfma_f32_16x16x32_bf16 %0, %3, %7, %0\n\t" \
  "v_mfma_f32_16x16x32_bf16 %0, %4, %8, %0\n\t" \
  "s_nop 7\n\ts_nop 1"
template <bool A> FI void pv_tile(Soft& st, const v2u& p, const Tile& v, const Masks& k) {
  const v4u b0 = diag(dup_lo(p[0]), k), b1 = diag(dup_hi(p[0]), k), b2 = diag(dup_lo(p[1]), k), b3 = diag(dup_hi(p[1]), k);
  if constexpr (A)
    asm volatile(PV_MFMAS : "+v"(st.acc) : "a"(v.r[0]), "a"(v.r[1]), "a"(v.r[2]), "a"(v.r[3]), "v"(b0), "v"(b1), "v"(b2), "v"(b3));
  else
    asm volatile(PV_MFMAS : "+v"(st.acc) : "v"(v.r[0]), "v"(v.r[1]), "v"(v.r[2]), "v"(v.r[3]), "v"(b0), "v"(b1), "v"(b2), "v"(b3));
}

constexpr bool vr_in_a(int i) { return i < 7; }

constexpr int imin(int a, int b) { return a < b ? a : b; }
constexpr int imax(int a, int b) { return a > b ? a : b; }

constexpr int AUX_SC1 = 16;

__global__ void __launch_bounds__(256, 1)
fused_decode_kernel(const unsigned short* __restrict__ xin, const unsigned char* __restrict__ kc, const unsigned char* __restrict__ vc,
                    const unsigned char* __restrict__ wq, const unsigned char* __restrict__ wo, const unsigned char* __restrict__ wg,
                    const unsigned char* __restrict__ wu, const unsigned char* __restrict__ wd, unsigned short* __restrict__ out,
                    unsigned char* ws) {
  __shared__ __attribute__((aligned(16))) unsigned char smem[L_BYTES];
  const int b = blockIdx.x;
  const int w = __builtin_amdgcn_readfirstlane(threadIdx.x >> 6);
  const int head = b >> 4, split = b & 15;
  const int myr = b - (NWG - NRED);
  const unsigned lbase = (unsigned)(size_t)smem;

  const v4i rsK = mk_rsrc(kc + ((size_t)head << 24), 1u << 24);
  const v4i rsV = mk_rsrc(vc + ((size_t)head << 24), 1u << 24);
  const v4i rsQ = mk_rsrc(wq, DM * NQ * 2);
  const v4i rsC = mk_rsrc(ws, WS_BYTES);
  const __amdgpu_buffer_rsrc_t rsW = __builtin_amdgcn_make_buffer_rsrc(ws, 0, WS_BYTES, 0x00020000);

  const unsigned m0base = lbase + L_KS + w * (NL * 4096);
  const unsigned soffw = split * 16384 + w * 4096;
  auto toff = [&](unsigned t) { return soffw + (t << 18); };
  int lane, g, c, tid;
  Masks mk;
  unsigned voffV, voffK, addrK;
  auto relane = [&]() {
    const Ln L = make_ln();
    lane = L.lane; g = L.g; c = L.c; mk = L.mk; tid = w * 64 + lane;
    voffV = lane * 16;
    voffK = g * 256 + ((c ^ g) << 4);
    const unsigned kap = 4 * (c & 3) + (c >> 2);
    addrK = m0base + kap * 256 + (((4 * g) ^ kap) << 4);
  };
  relane();

  Tile KR[NR], VR[NR], VL[NL];

  auto now8 = [&]() { return (unsigned)__builtin_amdgcn_s_memrealtime() << 8; };
  unsigned t_go = 0, due = 0;
  auto gate = [&](unsigned pace) {
    due += pace;
    while ((int)(now8() - due) < 0) __builtin_amdgcn_s_sleep(1);
  };
  auto send_k = [&](int slot, unsigned t) { issue_k(m0base + slot * 4096, voffK, rsK, toff(t)); };

  const int qs = (b & 7) * 32 + (b >> 3);
  v2u wqf[12];
  auto issue_wq = [&]() {
    const unsigned voffQ = (192u * w + (4 * g + (c >> 2)) * 12u) * (NQ * 2) + (unsigned)(qs * 16 + 4 * (c & 3)) * 2u;
    sfor<0, 12>([&](auto I) {
      load_b64(wqf[I.value], voffQ, rsQ, I.value * NQ * 2);
    });
  };

  auto xpiece = [&](int np, int k) { const unsigned j = np * w + lane + 64 * k; return j >= 144u ? j - 144u : j; };
  auto layernorm = [&](v2u (&px)[3], int np) {
    const bool act2 = lane < 16;
    auto up = [](unsigned pr) { return (f2){bf_lo(pr), bf_hi(pr)}; };
    const float x0 = bf_lo(__builtin_amdgcn_readfirstlane(px[0][0]));
    f2 s = {0.f, 0.f}, q = {0.f, 0.f};
#pragma unroll
    for (int k = 0; k < 3; ++k) {
#pragma unroll
      for (int i = 0; i < 2; ++i) {
        f2 a = up(px[k][i]) - x0;
        if (k == 2) a = act2 ? a : (f2){0.f, 0.f};
        s += a;
        q = __builtin_elementwise_fma(a, a, q);
      }
    }
    float sq[2] = {s[0] + s[1], q[0] + q[1]};
    row_sums(sq);
    xrow_sums2(sq[0], sq[1]);
    const float md = sq[0] * (1.f / DM);
    const float mean = x0 + md;
    const float var = fmaxf(__builtin_fmaf(-md, md, sq[1] * (1.f / DM)), 0.f);
    const float mb = rbf(mean);
    const float den = rbf(__builtin_amdgcn_sqrtf(rbf(rbf(var) + 1e-5f)));
    const float rc = __builtin_amdgcn_rcpf(den);
    auto dv = [&](unsigned pr) {
      const unsigned a2 = pk_bf(bf_lo(pr) - mb, bf_hi(pr) - mb);
      const f2 a = {bf_lo(a2), bf_hi(a2)};
      const f2 q0 = a * rc;
      const f2 q1 = __builtin_elementwise_fma(__builtin_elementwise_fma(-q0, (f2){den, den}, a), (f2){rc, rc}, q0);
      return pk_bf(q1[0], q1[1]);
    };
    v2u o = {dv(px[0][0]), dv(px[0][1])};
    if (lane < np) *(v2u*)(smem + L_XB + (np * w + lane) * 8) = o;
  };

  auto q_gemv = [&]() {
    if (w < 3) {
      const v2u* xp = (const v2u*)(smem + L_XB + (192 * w + (4 * g + (c >> 2)) * 12) * 2);
      const v2u xa = xp[0], xb = xp[1], xc = xp[2];
      const unsigned xs[6] = {xa[0], xa[1], xb[0], xb[1], xc[0], xc[1]};
      const unsigned ma = mk.m0 | mk.m2, mb = mk.m1 | mk.m3;
      f4 d;
#pragma unroll
      for (int n = 0; n < 6; ++n) {
        const unsigned lo = dup_lo(xs[n]), hi = dup_hi(xs[n]);
        const v4u bb = {lo & ma, lo & mb, hi & ma, hi & mb};
        const v4u aa = {wqf[2 * n][0], wqf[2 * n][1], wqf[2 * n + 1][0], wqf[2 * n + 1][1]};
        if (n == 0) d = mfma_first(aa, bb); else mfma_acc(d, aa, bb);
      }
      mfma_fence(d);
      if ((c >> 2) == g) {
        float* rp = (float*)(smem + L_REDA) + (w * 4 + g) * 16 + (c & 3);
#pragma unroll
        for (int r = 0; r < 4; ++r) rp[4 * r] = d[r];
      }
    }
  };
  auto q_publish = [&](unsigned tag) {
    const f2* rp = (const f2*)(smem + L_REDA) + (lane & 7);
    f2 a[12];
#pragma unroll
    for (int i = 0; i < 12; ++i) a[i] = rp[8 * i];
    const f2 t = (((a[0] + a[1]) + (a[2] + a[3])) + ((a[4] + a[5]) + (a[6] + a[7]))) + ((a[8] + a[9]) + (a[10] + a[11]));
    v2u gr = {pk_bf(t[0], t[1]), tag};
    __builtin_amdgcn_raw_buffer_store_b64(gr, rsW, lane < 8 ? WS_QG + (qs * 8 + lane) * 8 : OOB, 0, AUX_SC1);
  };
  auto q_keep = [&](const v4u& v) { v2u o = {v[0], v[2]}; *(v2u*)(smem + L_QB + lane * 8) = o; };

  unsigned long long pc;
  unsigned code_len;
  asm volatile("s_getpc_b64 %0\n.Lfd_code_from:\n\ts_mov_b32 %1, .Lfd_code_to-.Lfd_code_from" : "=s"(pc), "=s"(code_len));
  auto warm_code = [&]() { touch(lbase + L_PAD, lane * 64, mk_rsrc((const void*)pc, code_len), (b >> 3) * 4096); };
  auto put_tb = [&](unsigned tag, unsigned t) {
    if (b >= NWG - 8 && lane == 0) { v2u tb = {t, tag}; __builtin_amdgcn_raw_buffer_store_b64(tb, rsW, WS_TB + (b & 7) * 8, 0, AUX_SC1); }
  };
  auto keep_tb = [&](const v4u& tb) {
    int d = max((int)(tb[0] - t_go), (int)(tb[2] - t_go));
    d = max(d, __builtin_amdgcn_mov_dpp(d, 0xB1, 0xf, 0xf, true));
    d = max(d, __builtin_amdgcn_mov_dpp(d, 0x4E, 0xf, 0xf, true));
    lds_put(lbase + L_FLAG + 28, t_go + d);
  };

  unsigned tagbase;
  auto TAG = [&](unsigned ph) { return 0x80000000u | ((tagbase + ph) & 0x7fffffffu); };
  auto lds_wait = [&](int word) { for (unsigned i = 0; i < 8u * SPIN_CAP && lds_get(lbase + L_FLAG + 4 * word) == 0u; ++i) __builtin_amdgcn_s_sleep(1); };
  auto head_step = [&](auto Qi) {
    constexpr int q = Qi.value;
    if constexpr (q == D) { if (w != 3) { wait_vm<4 * (D - 1) + 12>(); t_go = now8(); } due = t_go + PACE_OFF1 * 256; }
    if constexpr (q >= D) wait_vm<4 * (D - 1) + (q - D < D ? 12 : 0)>();
    gate(PACE_R);
    if constexpr (q < NR) send_k(q, q);
    else if constexpr (q < 2 * NR) { issue_v<vr_in_a(q - NR) ? 0 : 1>(VR[q - NR], voffV, rsV, toff(q - NR)); read_k<q - NR>(KR[q - NR], addrK); }
    else send_k(q - 2 * NR, q - NR);
  };
  {
    v2u px[3];
    unsigned ep;
    send_k(0, 0);
    __builtin_amdgcn_sched_barrier(0);
    {
      const v4u z = {0u, 0u, 0u, 0u};
      if (tid < 144) __builtin_amdgcn_raw_buffer_store_b128(z, rsW, WS_PA + (head * 16 + tid / 9) * PA_STRIDE + split * PA_MSG + (tid % 9) * 16, 0, AUX_SC1);
      __builtin_amdgcn_raw_buffer_store_b128(z, rsW, WS_PB + (tid >> 3) * PB_STRIDE + b * 128 + (tid & 7) * 16, 0, AUX_SC1);
      if (tid < 32) __builtin_amdgcn_raw_buffer_store_b128(z, rsW, WS_PB + (32 + (tid >> 3)) * PB_STRIDE + b * 128 + (tid & 7) * 16, 0, AUX_SC1);
      if (myr >= 0 && tid < 4 * XREP) __builtin_amdgcn_raw_buffer_store_b128(z, rsW, WS_XG + (tid >> 2) * XG_STRIDE + myr * 64 + (tid & 3) * 16, 0, AUX_SC1);
      asm volatile("" ::: "memory");
    }
    const v4i rsX = mk_rsrc(xin, DM * 2);
    const v4i rsE = mk_rsrc(ws, 64);
    asm volatile("buffer_load_dwordx2 %0, %4, %7, 0 offen\n\t"
                 "buffer_load_dwordx2 %1, %5, %7, 0 offen\n\t"
                 "buffer_load_dwordx2 %2, %6, %7, 0 offen\n\t"
                 "buffer_load_dword %3, off, %8, 0 sc1"
                 : "=&v"(px[0]), "=&v"(px[1]), "=&v"(px[2]), "=&v"(ep)
                 : "v"(xpiece(48, 0) * 8), "v"(xpiece(48, 1) * 8), "v"(lane < 16 ? xpiece(48, 2) * 8 : OOB), "s"(rsX), "s"(rsE) : "memory");
    sfor<1, D>([&](auto I) { send_k(I.value, I.value); });
    issue_wq();
    if (w == 2) warm_code();
    if (tid < 8) *(unsigned*)(smem + L_FLAG + 4 * tid) = 0u;
    wg_barrier();
    if (w == 3) {
      asm volatile("s_waitcnt vmcnt(%4)" : "+v"(px[0]), "+v"(px[1]), "+v"(px[2]), "+v"(ep) : "i"(4 * (D - 1) + 12) : "memory");
      tagbase = __builtin_amdgcn_readfirstlane(ep);
      t_go = now8();
      put_tb(TAG(12), t_go);
#pragma unroll
      for (int k = 0; k < 3; ++k) if (((lane + 64 * k) >> 2) == myr) *(v2u*)(smem + L_XR + (lane & 3) * 8) = px[k];
    }
    sfor<D, QG_AT>(head_step);
    wait_wq<4 * (QG_AT - D)>(wqf);
    asm volatile("" : "+v"(ep), "+v"(px[0]), "+v"(px[1]), "+v"(px[2]));
    tagbase = __builtin_amdgcn_readfirstlane(ep);
    if (w < 3) layernorm(px, 48);
  }
  if (w < 3) {
    q_gemv();
    lds_put(lbase + L_FLAG + 4 + 4 * w, 1u);
  }
  v4u ride;
  def_v(ride);
  if (w == 3) ride_load(ride, rsC, WS_TB + (lane & 3) * 16);
  sfor<QG_AT, QG_AT + D + 1>(head_step);
  if (w == 3) {
    v4u a[3];
    a[0] = ride_take(ride);
    if (untagged(a[0], TAG(12)) != 0ull) poll3(a, rsC, WS_TB + (lane & 3) * 16, WS_TB + (lane & 3) * 16, OOB, TAG(12), 0ull);
    keep_tb(a[0]);
    lds_put(lbase + L_FLAG + 20, 1u);
    lds_wait(1); lds_wait(2); lds_wait(3);
    q_publish(TAG(1));
  }
  lds_wait(5);
  due += lds_get(lbase + L_FLAG + 28) + TB_DELTA * 256 - t_go;
  sfor<QG_AT + D + 1, 2 * NR - 2>(head_step);
  if (w == 3) ride_load(ride, rsC, WS_QG + head * 1024 + lane * 16);
  sfor<2 * NR - 2, 2 * NR + D>(head_step);
  if (w == 3) {
    v4u a[3];
    a[0] = ride_take(ride);
    if (untagged(a[0], TAG(1)) != 0ull) poll3(a, rsC, WS_QG + head * 1024 + lane * 16, WS_QG + head * 1024 + lane * 16, OOB, TAG(1), 0ull);
    q_keep(a[0]);
    lds_put(lbase + L_FLAG + 16, 1u);
  }
  lds_wait(4);

  Tile Q;
  Soft st, st2;
  unsigned moved;
  auto load_q = [&]() {
    const v4u* qp = (const v4u*)(smem + L_QB + (c >> 3) * 256 + g * 64);
#pragma unroll
    for (int j = 0; j < 4; ++j) Q.r[j] = qp[j];
    st.acc = (f4){0.f, 0.f, 0.f, 0.f};
    st.mc = -__builtin_inff();
    st.l = 0.f;
    st2 = st;
  };
  auto merge_st2 = [&]() {
    const float m = fmaxf(st.mc, st2.mc), a1 = ex2(st.mc - m), a2 = ex2(st2.mc - m);
    st.acc = st.acc * a1 + st2.acc * a2;
    st.l = st.l * a1 + st2.l * a2;
    st.mc = m;
  };
  auto tile_r = [&](Soft& s, auto I) { pv_tile<vr_in_a(I.value)>(s, softmax_tile<false>(s, qk(KR[I.value], Q), nullptr, moved, 0u), VR[I.value], mk); };
  auto tile_l = [&](Soft& s, auto I) {
    Tile kt;
    read_k<I.value>(kt, addrK);
    pv_tile<true>(s, softmax_tile<false>(s, qk(kt, Q), nullptr, moved, 0u), VL[I.value], mk);
  };
  WoFrag wo1, wo2;
  auto load_wo = [&](WoFrag& x) {
    const __amdgpu_buffer_rsrc_t r = __builtin_amdgcn_make_buffer_rsrc((void*)wo, 0, DM * NQ * 2, 0x00020000);
    const unsigned p = (b * 16 + 4 * g) * (DM * 2);
#pragma unroll
    for (int n = 0; n < 4; ++n) x.f[n] = __builtin_amdgcn_raw_buffer_load_b128(r, p + n * (DM * 2) + 256 * w + 16 * c, 0, AUX_SC1);
#pragma unroll
    for (int n = 0; n < 2; ++n) x.c[n] = __builtin_amdgcn_raw_buffer_load_b128(r, p + (2 * n + (c >> 3)) * (DM * 2) + 1024 + 16 * (c & 7), 0, AUX_SC1);
    asm volatile("" ::: "memory");
  };
  auto block = [&](auto NTc, auto PARKc, float* alp, unsigned t0, unsigned pace, bool more, auto&& side) {
    constexpr int NT = NTc.value;
    constexpr bool PARK = PARKc.value;
    v2u pend[NT];
    moved = 0u;
    sfor<0, 2 * NT>([&](auto Sc) {
      constexpr int s = Sc.value, q = s + D;
      if constexpr (s + D - 1 < 2 * NT) wait_vm<4 * (D - 1)>();
      else { if (more) wait_vm<4 * (D - 1)>(); else wait_vm<4 * (2 * NT - 1 - s)>(); }
      if constexpr (s < NT) {
        Tile kt;
        read_k<s % D>(kt, addrK);
        if constexpr (q < NT) { gate(pace); send_k(q % D, t0 + q); }
        else { gate(pace); issue_v<2>(VL[(q - NT) % D], voffV, rsV, toff(t0 + q - NT)); }
        const v2u pp = softmax_tile<true>(st, qk(kt, Q), alp + s * 64, moved, 1u << s);
        pend[s][0] = park<PARK>(pp[0]); pend[s][1] = park<PARK>(pp[1]);
      } else {
        constexpr int j = s - NT;
        if (moved & (1u << j)) st.acc *= alp[j * 64];
        const v2u pp = {unpark<PARK>(pend[j][0]), unpark<PARK>(pend[j][1])};
        pv_tile<true>(st, pp, VL[j % D], mk);
        if constexpr (q < 2 * NT) { gate(pace); issue_v<2>(VL[(q - NT) % D], voffV, rsV, toff(t0 + q - NT)); }
        else if (more) { gate(pace); send_k(q - 2 * NT, t0 + NT + q - 2 * NT); }
      }
      side(Sc);
    });
  };
  using I0 = std::integral_constant<int, 0>;
  using I1 = std::integral_constant<int, 1>;

  auto warm = [&](int wi) {
    const v4i rsG = mk_rsrc(wg, DM * DFF * 2), rsU = mk_rsrc(wu, DM * DFF * 2);
    const unsigned q = (b >> 3) * 1536u + tid + 256 * wi, r = q / 72u;
    const unsigned vg = r * (DFF * 2) + (q - r * 72u) * 16u;
    touch16(lbase + L_PAD, vg, rsG, (b & 7) * 1152u);
    touch16(lbase + L_PAD, vg, rsU, (b & 7) * 1152u);
  };
  auto chain = [&](auto LAYER, const WoFrag& wf) {
    constexpr int layer = LAYER.value;
    relane();
    const unsigned ph = 2 + 5 * layer;

    const int mu = (b & 7) * 18 + (b >> 3);
    constexpr int GW = layer == 0 ? GATE_WIN : 27;
    v4u wb[GW];
    const unsigned char* pgu;
    unsigned odn, odc;
    auto bases = [&]() {
      pgu = (((c >> 2) & 1) ? wu : wg) + (size_t)(144 * w + (2 * g + (c >> 3)) * 18) * (DFF * 2) + 64 * mu + 16 * (c & 3);
      odn = (32 * mu + 8 * w + 2 * g) * (DM * 2) + 16 * c;
      odc = (32 * mu + 8 * w + 2 * g + (c >> 3)) * (DM * 2) + 1024 + 16 * (c & 7);
    };
    const __amdgpu_buffer_rsrc_t rsDn = __builtin_amdgcn_make_buffer_rsrc((void*)wd, 0, DM * DFF * 2, 0x00020000);
    auto frag = [&](auto F) {
      constexpr int f = F.value;
      if constexpr (f < 18) wb[f % GW] = *(const v4u*)(pgu + (size_t)f * (DFF * 2));
      else if constexpr (f < 26) wb[f % GW] = __builtin_amdgcn_raw_buffer_load_b128(rsDn, odn + ((f - 18) & 1) * (DM * 2) + ((f - 18) >> 1) * 256, 0, AUX_SC1);
      else if constexpr (f == 26) wb[f % GW] = __builtin_amdgcn_raw_buffer_load_b128(rsDn, odc, 0, AUX_SC1);
    };
    {
      const float lt = xrow_sum(st.l);
      float* cp = (float*)(smem + L_REDB) + w * (NTA * 64);
#pragma unroll
      for (int r = 0; r < 4; ++r) cp[(c >> 3) * 128 + 32 * g + 8 * r + (c & 7)] = st.acc[r];
      if ((lane & 0x37) == 0) { f2 ml = {st.mc, lt}; *(f2*)(cp + 256 + 2 * (c >> 3)) = ml; }
    }
    wg_barrier();
    {
      const float* cp = (const float*)(smem + L_REDB);
      const int h = tid >> 7;
      f2 ml[4];
#pragma unroll
      for (int i = 0; i < 4; ++i) ml[i] = *(const f2*)(cp + i * (NTA * 64) + 256 + 2 * h);
      const float M = fmaxf(fmaxf(ml[0][0], ml[1][0]), fmaxf(ml[2][0], ml[3][0]));
      float O = 0.f, L = 0.f;
#pragma unroll
      for (int i = 0; i < 4; ++i) { const float sc = ex2(ml[i][0] - M); O += sc * cp[i * (NTA * 64) + tid]; L += sc * ml[i][1]; }
      const unsigned tg = TAG(ph);
      const unsigned pa = WS_PA + (head * 16 + (tid >> 4)) * PA_STRIDE + split * PA_MSG;
      v2u gr = {__float_as_uint(O), tg};
      __builtin_amdgcn_raw_buffer_store_b64(gr, rsW, pa + c * 8, 0, AUX_SC1);
      if (c == 0) {
        v4u g2 = {__float_as_uint(M), tg, __float_as_uint(L), tg};
        __builtin_amdgcn_raw_buffer_store_b128(g2, rsW, pa + 128, 0, AUX_SC1);
      }
    }
    relane();
    {
      const unsigned tg = TAG(ph);
      const unsigned base = WS_PA + b * PA_STRIDE + c * PA_MSG;
      v4u a[3];
      const unsigned tb = TAG(ph + 1);
      const bool hp = (c >> 3) != 0, odd = (c & 1) != 0;
      const unsigned lo_ = WS_PB + b * 128 + (odd ? 64 : 0) + (c & 6) * 8;
      unsigned ob1 = lo_ + (8 * w + 2 * g + (hp ? 1 : 0)) * PB_STRIDE, ob2 = g == (w >> 1) && !hp ? lo_ + (32 + w) * PB_STRIDE : OOB;
      pin(mk); pin(ob1); pin(ob2);
      __builtin_amdgcn_s_sleep(6);
      poll3(a, rsC, base + 32 * g, base + 32 * g + 16, base + 128, tg, ~0ull);
      const float ms = __uint_as_float(a[2][0]);
      const float wgt = ex2(ms - row_max(ms));
      float o[5] = {wgt * __uint_as_float(a[0][0]), wgt * __uint_as_float(a[0][2]), wgt * __uint_as_float(a[1][0]), wgt * __uint_as_float(a[1][2]),
                    wgt * __uint_as_float(a[2][2])};
      row_sums(o);
      const float inv = frcp(o[4]);
#pragma unroll
      for (int n = 0; n < 4; ++n) o[n] *= inv;
      auto put_line = [&](float lo, float hi, unsigned off) {
        const float lon = dpp<0xB1>(lo), hin = dpp<0xB1>(hi);
        v4u gr = {__float_as_uint(odd ? hin : lo), tb, __float_as_uint(odd ? hi : lon), tb};
        __builtin_amdgcn_raw_buffer_store_b128(gr, rsW, off, 0, AUX_SC1);
      };
      const float oa = hp ? o[1] : o[0], obv = hp ? o[3] : o[2];
      f4 e = mfma_first(wf.c[0], diag(pk_bf(oa, oa), mk));
      mfma_acc(e, wf.c[1], diag(pk_bf(obv, obv), mk));
      f4 d = mfma_first(wf.f[0], diag(pk_bf(o[0], o[0]), mk));
#pragma unroll
      for (int n = 1; n < 4; ++n) mfma_acc(d, wf.f[n], diag(pk_bf(o[n], o[n]), mk));
      mfma_fence2(d, e);
      put_line(hp ? d[2] : d[0], hp ? d[3] : d[1], ob1);
      {
        const float lo = (w & 1) ? e[2] : e[0], hi = (w & 1) ? e[3] : e[1];
        float f[2] = {(g >> 1) == (hp ? 1 : 0) ? lo : 0.f, (g >> 1) == (hp ? 1 : 0) ? hi : 0.f};
#pragma unroll
        for (int r = 0; r < 2; ++r) {
          const float t = f[r] + dpp<0x128>(f[r]);
          auto sw = __builtin_amdgcn_permlane32_swap(__float_as_uint(t), __float_as_uint(t), false, false);
          f[r] = __uint_as_float(sw[0]) + __uint_as_float(sw[1]);
        }
        put_line(f[0], f[1], ob2);
      }
    }
    relane();
    if (layer == 0 && myr < 0) {
#pragma unroll
      for (int i = WARM1; i < 6; ++i) warm(i);
      if (w == 2) warm_code();
    }
    auto reduce_cols = [&](auto NPc, unsigned tg, auto FINAL, unsigned tgx) {
      constexpr int NP = NPc.value, NK = (NP * 8 + 255) / 256;
      v4u a[8];
      unsigned long long miss[8];
#pragma unroll
      for (int k = 0; k < 8; ++k) { a[k] = (v4u){0u, 0u, 0u, 0u}; miss[k] = __builtin_amdgcn_ballot_w64(tid + 256 * k < NP * 8); }
      const unsigned base = WS_PB + myr * PB_STRIDE;
      v2u xo = *(const v2u*)(smem + L_XR + (tid & 3) * 8);
      unsigned ox = FINAL.value ? (tid < 4 ? 32 * myr + 8 * tid : OOB) : (tid < 4 * XREP ? WS_XG + (tid >> 2) * XG_STRIDE + myr * 64 + (tid & 3) * 16 : OOB);
      unsigned x0 = xo[0], x1 = xo[1], ra = L_REDA + (tid & 3) * 16;
      pin(x0); pin(x1); pin(ox); pin(ra);
      if constexpr (NP == NWG) __builtin_amdgcn_s_sleep(12);
#pragma unroll 1
      for (unsigned spins = 0; spins < SPIN_CAP; ++spins) {
        ldm4(a[0], a[1], a[2], a[3], rsC, tid * 16, base, miss[0], miss[1], miss[2], miss[3]);
        if ((miss[4] | miss[5] | miss[6] | miss[7]) != 0ull) ldm4(a[4], a[5], a[6], a[7], rsC, tid * 16, base + 16384, miss[4], miss[5], miss[6], miss[7]);
        pin4(a[4], a[5], a[6], a[7]);
        pin4(a[0], a[1], a[2], a[3]);
        unsigned long long any = 0ull;
#pragma unroll
        for (int k = 0; k < NK; ++k) { miss[k] &= untagged(a[k], tg); any |= miss[k]; }
        if (any == 0ull) break;
      }
      float s0 = __uint_as_float(a[0][0]), s1 = __uint_as_float(a[0][2]);
#pragma unroll
      for (int k = 1; k < NK; ++k) { s0 += __uint_as_float(a[k][0]); s1 += __uint_as_float(a[k][2]); }
      s0 += dpp<0x128>(s0); s1 += dpp<0x128>(s1);
      xrow_sums2(s0, s1);
      if (lane < 8) ((f2*)(smem + L_REDA))[w * 8 + lane] = (f2){s0, s1};
      wg_barrier();
      if (w == 0) {
        const f4* rp = (const f4*)(smem + ra);
        const f4 t = (rp[0] + rp[4]) + (rp[8] + rp[12]);
        const unsigned t0 = pk_bf(t[0], t[1]), t1 = pk_bf(t[2], t[3]);
        const v2u xn = {pk_bf(bf_lo(x0) + bf_lo(t0), bf_hi(x0) + bf_hi(t0)), pk_bf(bf_lo(x1) + bf_lo(t1), bf_hi(x1) + bf_hi(t1))};
        if constexpr (FINAL.value) {
          __builtin_amdgcn_raw_buffer_store_b64(xn, __builtin_amdgcn_make_buffer_rsrc(out, 0, DM * 2, 0x00020000), ox, 0, 0);
        } else {
          const v4u gx = {xn[0], tgx, xn[1], tgx};
          __builtin_amdgcn_raw_buffer_store_b128(gx, rsW, ox, 0, AUX_SC1);
          if (tid < 4) *(v2u*)(smem + L_XR + tid * 8) = xn;
        }
      }
    };
    auto gather_x_ln = [&](unsigned tg, int np) {
      v4u a[3];
      const unsigned xb = WS_XG + (b % XREP) * XG_STRIDE;
      poll3(a, rsC, xb + xpiece(np, 0) * 16, xb + xpiece(np, 1) * 16, lane < 16 ? xb + xpiece(np, 2) * 16 : OOB, tg, 0xffffull);
      v2u px[3];
#pragma unroll
      for (int k = 0; k < 3; ++k) { px[k][0] = a[k][0]; px[k][1] = a[k][2]; }
      layernorm(px, np);
    };

    if (b < NMLP) {
      bases();
      if constexpr (layer == 0) wait_vm<0>();
      sfor<0, GW>(frag);
      unsigned xoff = L_XB + (144 * w + (2 * g + (c >> 3)) * 18) * 2;
      if constexpr (layer != 0) { pin(mk); pin(xoff); }
      gather_x_ln(TAG(ph + 2), 36);
      if constexpr (layer == 0) { relane(); bases(); xoff = L_XB + (144 * w + (2 * g + (c >> 3)) * 18) * 2; }
      f4 d;
      {
        const unsigned* xp = (const unsigned*)(smem + xoff);
        unsigned xs[9];
        constexpr int XA = layer == 0 ? 4 : 9;
#pragma unroll
        for (int n = 0; n < XA; ++n) xs[n] = xp[n];
        sfor<0, 9>([&](auto Nn) {
          constexpr int n = Nn.value;
          const v4u b0 = diag(dup_lo(xs[n]), mk), b1 = diag(dup_hi(xs[n]), mk);
          if constexpr (n + XA < 9) xs[n + XA] = xp[n + XA];
          if constexpr (n == 0) d = mfma_first2(wb[0], b0, wb[1], b1); else mfma_acc2(d, wb[(2 * n) % GW], b0, wb[(2 * n + 1) % GW], b1);
          if constexpr (layer == 0) __builtin_amdgcn_sched_barrier(0);
          frag(std::integral_constant<int, 2 * n + GW>{});
          frag(std::integral_constant<int, 2 * n + 1 + GW>{});
          if constexpr (layer == 0) __builtin_amdgcn_sched_barrier(0);
        });
      }
      const bool hp = (c >> 3) != 0;
      unsigned ra = L_REDA + ((w * 4 + g) * 32 + (c & 7)) * 4, rb = L_REDA + (4 * w + g + 32 * (c & 7) + 16 * (c >> 3)) * 8;
      unsigned rc_ = L_REDB + (w * 512 + 32 * g + (c & 7)) * 4, rd = L_REDB + (2048 + w * 128 + 32 * g + (c & 7)) * 4;
      unsigned o1 = WS_PB + (tid >> 3) * PB_STRIDE + mu * 128 + (tid & 7) * 16, o2 = lane < 8 ? WS_PB + (32 + w) * PB_STRIDE + mu * 128 + lane * 16 : OOB;
      pin(ra); pin(rb); pin(rc_); pin(rd); pin(o1); pin(o2);
      mfma_fence(d);
      if ((g >> 1) == (c >> 3)) {
        float* rp = (float*)(smem + ra);
#pragma unroll
        for (int r = 0; r < 4; ++r) rp[8 * r] = d[r];
      }
      wg_barrier();
      unsigned hd0, hd1;
      {
        const f2 v = *(const f2*)(smem + rb);
        float t[2] = {v[0], v[1]};
#pragma unroll
        for (int i = 0; i < 2; ++i) t[i] += dpp<0xB1>(t[i]);
#pragma unroll
        for (int i = 0; i < 2; ++i) t[i] += dpp<0x4E>(t[i]);
#pragma unroll
        for (int i = 0; i < 2; ++i) t[i] += dpp<0x141>(t[i]);
        const float y = dpp<0x128>(hp ? t[0] : t[1]), own = hp ? t[1] : t[0];
        const float x = rbf(hp ? y : own), up = rbf(hp ? own : y);
        const float z = 0.7978845608028654f * (x + 0.044715f * x * x * x);
        const float th = 1.f - 2.f * __builtin_amdgcn_rcpf(__expf(2.f * z) + 1.f);
        const float hv = rbf(0.5f * x * (1.f + th)) * up;
        const unsigned hd = pk_bf(hv, hv), ho = __builtin_amdgcn_mov_dpp(hd, 0x128, 0xf, 0xf, true);
        hd0 = hp ? ho : hd; hd1 = hp ? hd : ho;
        f4 e[5];
        const v4u b0 = diag(hd0, mk), b1 = diag(hd1, mk);
#pragma unroll
        for (int bl = 0; bl < 4; ++bl) e[bl] = mfma_first2(wb[(18 + 2 * bl) % GW], b0, wb[(19 + 2 * bl) % GW], b1);
        e[4] = mfma_first(wb[26 % GW], diag(hd, mk));
        mfma_fence5(e);
        if (!hp) {
#pragma unroll
          for (int bl = 0; bl < 4; ++bl) {
            float* q = (float*)(smem + rc_) + 128 * bl;
#pragma unroll
            for (int r = 0; r < 4; ++r) q[8 * r] = e[bl][r];
          }
        }
        if ((g >> 1) == (c >> 3)) {
          float* q = (float*)(smem + rd);
#pragma unroll
          for (int r = 0; r < 4; ++r) q[8 * r] = e[4][r];
        }
      }
      wg_barrier();
      {
        const f2* rp = (const f2*)(smem + L_REDB);
        const unsigned tg = TAG(ph + 3);
        f2 m[4], x[8];
#pragma unroll
        for (int i = 0; i < 4; ++i) m[i] = rp[256 * i + tid];
#pragma unroll
        for (int i = 0; i < 8; ++i) x[i] = rp[1024 + 32 * i + 8 * w + (lane & 7)];
        const f2 a = (m[0] + m[1]) + (m[2] + m[3]);
        v4u g0 = {__float_as_uint(a[0]), tg, __float_as_uint(a[1]), tg};
        __builtin_amdgcn_raw_buffer_store_b128(g0, rsW, o1, 0, AUX_SC1);
        const f2 e = ((x[0] + x[1]) + (x[2] + x[3])) + ((x[4] + x[5]) + (x[6] + x[7]));
        v4u g1 = {__float_as_uint(e[0]), tg, __float_as_uint(e[1]), tg};
        __builtin_amdgcn_raw_buffer_store_b128(g1, rsW, o2, 0, AUX_SC1);
      }
      if constexpr (layer == 0) issue_wq();
    } else {
      if (myr >= 0) {
        reduce_cols(std::integral_constant<int, NWG>{}, TAG(ph + 1), std::false_type{}, TAG(ph + 2));
      }
      if constexpr (layer == 0) issue_wq();
      if (myr >= 0) {
        reduce_cols(std::integral_constant<int, NMLP>{}, TAG(ph + 3), std::integral_constant<bool, layer == 1>{}, TAG(ph + 4));
        if (layer == 1 && myr == 0 && tid == 0) __builtin_amdgcn_raw_buffer_store_b32(tagbase + 16u, rsW, WS_EPOCH, 0, AUX_SC1);
      }
    }
    if constexpr (layer == 0) {
      relane();
      pin(mk);
      if (w < 3) gather_x_ln(TAG(ph + 4), 48);
      if (w == 0) put_tb(TAG(ph + 5), now8());
      wait_wq<0>(wqf);
      q_gemv();
      wg_barrier();
      if (w == 3) {
        q_publish(TAG(ph + 5));
        __builtin_amdgcn_s_sleep(6);
        v4u a[3];
        poll3(a, rsC, WS_QG + head * 1024 + lane * 16, WS_TB + (lane & 3) * 16, OOB, TAG(ph + 5), 0ull);
        q_keep(a[0]);
        keep_tb(a[1]);
      }
      wg_barrier();
    }
  };

  load_q();
  sfor<0, D>([&](auto I) { def_a(VL[I.value]); });
  float* alp_ks = (float*)(smem + L_KS + w * (NL * 4096) + D * 4096) + lane;
#pragma unroll 1
  for (int i = 0; i < NB1; ++i) block(std::integral_constant<int, NT1>{}, std::true_type{}, alp_ks, NR + i * NT1, PACE_B1, true, [](auto) {});
  sfor<D, 2 * NL>([&](auto Qi) {
    constexpr int q = Qi.value;
    wait_vm<4 * (D - 1)>();
    if constexpr (q - D >= NL) tile_l(st, std::integral_constant<int, q - D - NL>{});
    gate(PACE_B1);
    if constexpr (q < NL) send_k(q, NTILE - NL + q);
    else issue_v<(q - NL < D) ? 2 : 0>(VL[q - NL], voffV, rsV, toff(NTILE - 2 * NL + q));
    if constexpr (q - D < NR) tile_r(st, std::integral_constant<int, q - D>{});
  });
#pragma unroll
  for (int i = 0; i < WARM1; ++i) warm(i);
  sfor<NL - D, NL>([&](auto I) {
    wait_vm<4 * (NL - 1 - I.value) + 2 * WARM1 + (I.value == NL - 1 ? 6 : 0)>();
    tile_l(st, I);
    if constexpr (I.value == NL - 2) load_wo(wo1);
  });
  chain(I0{}, wo1);
  relane();
  due = lds_get(lbase + L_FLAG + 28) + PACE_OFF2 * 256;
  load_q();
  sfor<0, D>([&](auto I) {
    tile_l(st2, I);
    if constexpr (I.value > 0) gate(PACE_A2);
    send_k(I.value, NR + I.value);
  });
  block(std::integral_constant<int, NTA>{}, std::false_type{}, (float*)(smem + L_REDB) + w * (NTA * 64) + lane, NR, PACE_A2, true, [&](auto S) {
    constexpr int nk = NR + NL - D, ink = nk - NTA, s = S.value;
    constexpr int k = s >= NTA ? ink + (s - NTA) : ((s * ink) / NTA != ((s + 1) * ink) / NTA ? (s * ink) / NTA : -1);
    if constexpr (k >= NR) tile_l(st2, std::integral_constant<int, k - NR + D>{});
    else if constexpr (k >= 0) tile_r(st2, std::integral_constant<int, k>{});
  });
  merge_st2();
  due += SLACK2 * 256;
  alp_ks = (float*)(smem + L_KS + w * (NL * 4096) + D * 4096) + lane;
#pragma unroll 1
  for (int i = 0; i < NB2; ++i) block(std::integral_constant<int, NT2>{}, std::false_type{}, alp_ks, NR + NTA + i * NT2, PACE_B2, i < NB2 - 1, [](auto) {});
  load_wo(wo2);
  chain(I1{}, wo2);
  asm volatile(".Lfd_code_to:");
}

}

extern "C" void kernel_launch(void* const* d_in, const int* in_sizes, int n_in, void* d_out, int out_size, void* d_ws, size_t ws_size,
                              hipStream_t stream) {
  (void)in_sizes; (void)n_in; (void)out_size; (void)ws_size;
  fused_decode_kernel<<<NWG, 256, 0, stream>>>(
      (const unsigned short*)d_in[0], (const unsigned char*)d_in[1], (const unsigned char*)d_in[2], (const unsigned char*)d_in[3],
      (const unsigned char*)d_in[4], (const unsigned char*)d_in[6], (const unsigned char*)d_in[7], (const unsigned char*)d_in[8],
      (unsigned short*)d_out, (unsigned char*)d_ws);
}
```

```cpp
#include <hip/hip_runtime.h>
#include <hip/hip_bf16.h>
#include <type_traits>

namespace {

typedef unsigned v4u __attribute__((ext_vector_type(4)));
typedef unsigned v2u __attribute__((ext_vector_type(2)));
typedef int v4i __attribute__((ext_vector_type(4)));
typedef float f4 __attribute__((ext_vector_type(4)));
typedef float f2 __attribute__((ext_vector_type(2)));
typedef __bf16 bf2 __attribute__((ext_vector_type(2)));
#define FI __device__ __forceinline__

constexpr int DM = 576, DFF = 4608, NQ = 4096;
constexpr int NWG = 256;
constexpr int NMLP = 144;
constexpr int NRED = 36;
constexpr int XREP = 8;
constexpr int NTILE = 64;
constexpr int NR = 9;
constexpr int NL = 9;
constexpr int D = 3;
constexpr int NSTREAM = NTILE - NR - NL, NTA = 10, NB1 = 2, NT1 = NSTREAM / NB1, NB2 = 2, NT2 = (NSTREAM - NTA) / NB2;
static_assert(NB1 * NT1 == NSTREAM && NTA + NB2 * NT2 == NSTREAM && D >= 2 && D <= 4 && NR + NL - D <= 2 * NTA && NT1 <= 32 && NT2 <= 32, "schedule");
static_assert(NT1 * 256 <= (NL - D) * 4096 && 2 * NT1 <= (NL - D) * 16, "room for the rescale factors and the parked probabilities");
static_assert(4 * XREP <= 64, "wave 0 writes every copy of x");
constexpr int GATE_WIN = 12;
constexpr int WARM1 = 2;
constexpr int QG_AT = 5;
constexpr unsigned PACE_R = 14464, PACE_B1 = 13888, PACE_A2 = 14208, PACE_B2 = 13888;
constexpr int PACE_OFF1 = -10, TB_DELTA = 40, PACE_OFF2 = 180, SLACK2 = 40;
static_assert(GATE_WIN >= 9, "all down fragments must fit the window");
constexpr float QSCALE = 0.08838834764831845f * 1.4426950408889634f;
constexpr unsigned SPIN_CAP = 1u << 21;

constexpr unsigned WS_EPOCH = 0;
constexpr unsigned WS_TB = 256;
constexpr unsigned WS_QG = 4096;
constexpr unsigned WS_XG = 32768, XG_STRIDE = 2560;
constexpr unsigned WS_PA = 65536 + 32768, PA_MSG = 160, PA_STRIDE = 16 * PA_MSG;
constexpr unsigned WS_PB = 1u << 20, PB_STRIDE = NWG * 128;
constexpr unsigned WS_BYTES = WS_PB + NRED * PB_STRIDE;
constexpr unsigned OOB = 0x7fffff00u;

constexpr int L_KS = 0;
constexpr int L_SC = 4 * NL * 4096;
constexpr int L_REDB = L_SC;
constexpr int L_REDA = L_SC + 10240;
constexpr int L_XB = L_REDA + 2048;
constexpr int L_QB = L_XB + 1280;
constexpr int L_PAD = L_QB + 512;
constexpr int L_FLAG = L_PAD + 1024;
constexpr int L_XR = L_FLAG + 32;
constexpr int L_BYTES = L_XR + 32;
static_assert(L_BYTES <= 163840, "LDS");

template <int B, int E, class F> FI void sfor(F&& f) {
  if constexpr (B < E) { f(std::integral_constant<int, B>{}); sfor<B + 1, E>(f); }
}

FI v4i mk_rsrc(const void* p, unsigned bytes) {
  unsigned long long a = (unsigned long long)p;
  v4i r = {(int)(unsigned)a, (int)((unsigned)(a >> 32) & 0xffffu), (int)bytes, 0x00020000};
  return r;
}
FI float bf_lo(unsigned u) { return __uint_as_float(u << 16); }
FI float bf_hi(unsigned u) { return __uint_as_float(u & 0xffff0000u); }
FI unsigned pk_bf(float a, float b) { f2 v = {a, b}; return __builtin_bit_cast(unsigned, __builtin_convertvector(v, bf2)); }
FI float rbf(float a) { return bf_lo(pk_bf(a, a)); }
FI float frcp(float x) { const float r = __builtin_amdgcn_rcpf(x); return __builtin_fmaf(__builtin_fmaf(-x, r, 1.f), r, r); }
FI float ex2(float x) { return __builtin_amdgcn_exp2f(x); }

template <int CTRL> FI float dpp(float x) {
  return __builtin_bit_cast(float, __builtin_amdgcn_mov_dpp(__builtin_bit_cast(int, x), CTRL, 0xf, 0xf, true));
}
FI float row_sum(float x) {
  x += dpp<0xB1>(x); x += dpp<0x4E>(x); x += dpp<0x141>(x); x += dpp<0x128>(x); return x;
}
FI float row_max(float x) {
  x = fmaxf(x, dpp<0xB1>(x)); x = fmaxf(x, dpp<0x4E>(x)); x = fmaxf(x, dpp<0x141>(x)); x = fmaxf(x, dpp<0x128>(x)); return x;
}
FI float xrow_max(float x) {
  auto s = __builtin_amdgcn_permlane16_swap(__float_as_uint(x), __float_as_uint(x), false, false);
  x = fmaxf(__uint_as_float(s[0]), __uint_as_float(s[1]));
  auto t = __builtin_amdgcn_permlane32_swap(__float_as_uint(x), __float_as_uint(x), false, false);
  return fmaxf(__uint_as_float(t[0]), __uint_as_float(t[1]));
}
FI float xrow_sum(float x) {
  auto s = __builtin_amdgcn_permlane16_swap(__float_as_uint(x), __float_as_uint(x), false, false);
  x = __uint_as_float(s[0]) + __uint_as_float(s[1]);
  auto t = __builtin_amdgcn_permlane32_swap(__float_as_uint(x), __float_as_uint(x), false, false);
  return __uint_as_float(t[0]) + __uint_as_float(t[1]);
}
FI float wave_sum(float x) { return xrow_sum(row_sum(x)); }
template <int N> FI void row_sums(float (&x)[N]) {
#pragma unroll
  for (int i = 0; i < N; ++i) x[i] += dpp<0xB1>(x[i]);
  __builtin_amdgcn_sched_barrier(0);
#pragma unroll
  for (int i = 0; i < N; ++i) x[i] += dpp<0x4E>(x[i]);
  __builtin_amdgcn_sched_barrier(0);
#pragma unroll
  for (int i = 0; i < N; ++i) x[i] += dpp<0x141>(x[i]);
  __builtin_amdgcn_sched_barrier(0);
#pragma unroll
  for (int i = 0; i < N; ++i) x[i] += dpp<0x128>(x[i]);
  __builtin_amdgcn_sched_barrier(0);
}
FI void xrow_sums2(float& a, float& b) {
  auto s = __builtin_amdgcn_permlane16_swap(__float_as_uint(a), __float_as_uint(b), false, false);
  const float t = __uint_as_float(s[0]) + __uint_as_float(s[1]);
  auto u = __builtin_amdgcn_permlane32_swap(__float_as_uint(t), __float_as_uint(t), false, false);
  const float v = __uint_as_float(u[0]) + __uint_as_float(u[1]);
  auto r = __builtin_amdgcn_permlane16_swap(__float_as_uint(v), __float_as_uint(v), false, false);
  a = __uint_as_float(r[0]); b = __uint_as_float(r[1]);
}
FI void pin(unsigned& a) { asm volatile("" : "+v"(a)); }

FI void wg_barrier() { asm volatile("s_waitcnt lgkmcnt(0)\n\ts_barrier" ::: "memory"); }
template <int N> FI void wait_vm() { asm volatile("s_waitcnt vmcnt(%0)" ::"i"(N > 63 ? 63 : N) : "memory"); }

FI void mfma_acc(f4& d, const v4u& a, const v4u& b) {
  asm("s_nop 1\n\tv_mfma_f32_16x16x32_bf16 %0, %1, %2, %0" : "+v"(d) : "v"(a), "v"(b));
}
FI f4 mfma_first(const v4u& a, const v4u& b) {
  f4 d;
  asm("s_nop 1\n\tv_mfma_f32_16x16x32_bf16 %0, %1, %2, 0" : "=&v"(d) : "v"(a), "v"(b));
  return d;
}
FI void mfma_acc2(f4& d, const v4u& a0, const v4u& b0, const v4u& a1, const v4u& b1) {
  asm("s_nop 1\n\tv_mfma_f32_16x16x32_bf16 %0, %1, %2, %0\n\tv_mfma_f32_16x16x32_bf16 %0, %3, %4, %0" : "+v"(d) : "v"(a0), "v"(b0), "v"(a1), "v"(b1));
}
FI f4 mfma_first2(const v4u& a0, const v4u& b0, const v4u& a1, const v4u& b1) {
  f4 d;
  asm("s_nop 1\n\tv_mfma_f32_16x16x32_bf16 %0, %1, %2, 0\n\tv_mfma_f32_16x16x32_bf16 %0, %3, %4, %0" : "=&v"(d) : "v"(a0), "v"(b0), "v"(a1), "v"(b1));
  return d;
}
FI void mfma_fence(f4& d) { asm("s_nop 7\n\ts_nop 1" : "+v"(d)); }

FI void mfma_fence2(f4& d, f4& e) { asm("s_nop 7\n\ts_nop 1" : "+v"(d), "+v"(e)); }
FI void mfma_fence5(f4 (&e)[5]) { asm("s_nop 7\n\ts_nop 1" : "+v"(e[0]), "+v"(e[1]), "+v"(e[2]), "+v"(e[3]), "+v"(e[4])); }

struct Masks { unsigned m0, m1, m2, m3; };
FI v4u diag(unsigned dup, const Masks& k) { v4u b = {dup & k.m0, dup & k.m1, dup & k.m2, dup & k.m3}; return b; }
FI void pin(Masks& k) { asm volatile("" : "+v"(k.m0), "+v"(k.m1), "+v"(k.m2), "+v"(k.m3)); }
FI unsigned dup_lo(unsigned x) { return __builtin_amdgcn_perm(x, x, 0x01000100u); }
FI unsigned dup_hi(unsigned x) { return __builtin_amdgcn_perm(x, x, 0x03020302u); }

struct Tile { v4u r[4]; };
struct WoFrag { v4u f[4], c[2]; };

struct Ln {
  int lane, g, c;
  Masks mk;
};
FI Ln make_ln() {
  Ln L;
  asm volatile("v_mbcnt_lo_u32_b32 %0, -1, 0\n\tv_mbcnt_hi_u32_b32 %0, -1, %0" : "=v"(L.lane));
  L.g = L.lane >> 4; L.c = L.lane & 15;
  const unsigned hm = (L.c & 1) ? 0xffff0000u : 0x0000ffffu;
  const int e2 = (L.c & 7) >> 1;
  L.mk.m0 = e2 == 0 ? hm : 0u; L.mk.m1 = e2 == 1 ? hm : 0u; L.mk.m2 = e2 == 2 ? hm : 0u; L.mk.m3 = e2 == 3 ? hm : 0u;
  return L;
}

FI void issue_k(unsigned m0v, unsigned voff, const v4i& rs, unsigned soff) {
  unsigned keep, tmp;
  asm volatile(
      "s_nop 1\n\t"
      "s_mov_b32 %0, m0\n\t"
      "s_mov_b32 m0, %2\n\t"
      "v_xor_b32 %1, 64, %3\n\t"
      "buffer_load_dwordx4 %3, %4, %5 offen nt lds\n\t"
      "buffer_load_dwordx4 %1, %4, %5 offen offset:1024 nt lds\n\t"
      "v_xor_b32 %1, 0x80, %3\n\t"
      "buffer_load_dwordx4 %1, %4, %5 offen offset:2048 nt lds\n\t"
      "v_xor_b32 %1, 0xc0, %3\n\t"
      "buffer_load_dwordx4 %1, %4, %5 offen offset:3072 nt lds\n\t"
      "s_mov_b32 m0, %0"
      : "=&s"(keep), "=&v"(tmp)
      : "s"(m0v), "v"(voff), "s"(rs), "s"(soff)
      : "memory");
}
#define V_LOADS \
  "s_nop 4\n\t" \
  "buffer_load_dwordx4 %0, %4, %5, %6 offen nt\n\t" \
  "buffer_load_dwordx4 %1, %4, %5, %6 offen offset:1024 nt\n\t" \
  "buffer_load_dwordx4 %2, %4, %5, %6 offen offset:2048 nt\n\t" \
  "buffer_load_dwordx4 %3, %4, %5, %6 offen offset:3072 nt"
template <int MODE> FI void issue_v(Tile& t, unsigned voff, const v4i& rs, unsigned soff) {
  if constexpr (MODE == 0)
    asm volatile(V_LOADS : "=a"(t.r[0]), "=a"(t.r[1]), "=a"(t.r[2]), "=a"(t.r[3]) : "v"(voff), "s"(rs), "s"(soff) : "memory");
  else if constexpr (MODE == 1)
    asm volatile(V_LOADS : "=&v"(t.r[0]), "=&v"(t.r[1]), "=&v"(t.r[2]), "=&v"(t.r[3]) : "v"(voff), "s"(rs), "s"(soff) : "memory");
  else
    asm volatile(V_LOADS ::"a"(t.r[0]), "a"(t.r[1]), "a"(t.r[2]), "a"(t.r[3]), "v"(voff), "s"(rs), "s"(soff) : "memory");
}
FI void def_a(Tile& t) { asm volatile("" : "=a"(t.r[0]), "=a"(t.r[1]), "=a"(t.r[2]), "=a"(t.r[3])); }
template <bool A> FI unsigned park(unsigned v) {
  if constexpr (A) { unsigned r; asm("v_accvgpr_write_b32 %0, %1" : "=a"(r) : "v"(v)); return r; } else return v;
}
template <bool A> FI unsigned unpark(unsigned r) {
  if constexpr (A) { unsigned v; asm("v_accvgpr_read_b32 %0, %1" : "=v"(v) : "a"(r)); return v; } else return r;
}
FI void touch(unsigned m0v, unsigned voff, const v4i& rs, unsigned soff) {
  unsigned keep;
  asm volatile(
      "s_mov_b32 %0, m0\n\t"
      "s_mov_b32 m0, %1\n\t"
      "s_nop 2\n\t"
      "buffer_load_dword %2, %3, %4 offen lds\n\t"
      "s_mov_b32 m0, %0"
      : "=&s"(keep)
      : "s"(m0v), "v"(voff), "s"(rs), "s"(soff)
      : "memory");
}
FI void touch16(unsigned m0v, unsigned voff, const v4i& rs, unsigned soff) {
  unsigned keep;
  asm volatile(
      "s_mov_b32 %0, m0\n\t"
      "s_mov_b32 m0, %1\n\t"
      "s_nop 2\n\t"
      "buffer_load_dwordx4 %2, %3, %4 offen lds\n\t"
      "s_mov_b32 m0, %0"
      : "=&s"(keep)
      : "s"(m0v), "v"(voff), "s"(rs), "s"(soff)
      : "memory");
}
FI void ride_load(const v4u& q, const v4i& rs, unsigned voff) {
  asm volatile("s_nop 4\n\tbuffer_load_dwordx4 %0, %1, %2, 0 offen sc1" ::"v"(q), "v"(voff), "s"(rs) : "memory");
}
FI v4u ride_take(const v4u& q) {
  v4u r;
  asm volatile("v_mov_b64 %0, %2\n\tv_mov_b64 %1, %3" : "=&v"(*(v2u*)&r), "=&v"(*((v2u*)&r + 1)) : "v"(*(const v2u*)&q), "v"(*((const v2u*)&q + 1)));
  return r;
}
FI void def_v(v4u& t) { asm volatile("" : "=v"(t)); }
FI void ldm3(v4u (&a)[3], const v4i& rs, unsigned v0, unsigned v1, unsigned v2, unsigned long long m) {
  unsigned long long keep;
  asm volatile(
      "s_mov_b64 %3, exec\n\t"
      "s_mov_b64 exec, %8\n\t"
      "s_nop 2\n\t"
      "buffer_load_dwordx4 %0, %5, %4, 0 offen sc1\n\t"
      "buffer_load_dwordx4 %1, %6, %4, 0 offen sc1\n\t"
      "buffer_load_dwordx4 %2, %7, %4, 0 offen sc1\n\t"
      "s_mov_b64 exec, %3\n\t"
      "s_waitcnt vmcnt(0)"
      : "+v"(a[0]), "+v"(a[1]), "+v"(a[2]), "=&s"(keep) : "s"(rs), "v"(v0), "v"(v1), "v"(v2), "s"(m) : "memory");
}
FI unsigned long long untagged(const v4u& a, unsigned tg) {
  return __builtin_amdgcn_ballot_w64(a[1] != tg) | __builtin_amdgcn_ballot_w64(a[3] != tg);
}
FI void poll3(v4u (&a)[3], const v4i& rs, unsigned v0, unsigned v1, unsigned v2, unsigned tg, unsigned long long care2) {
#pragma unroll
  for (int k = 0; k < 3; ++k) a[k] = (v4u){0u, 0u, 0u, 0u};
  unsigned long long miss = ~0ull;
#pragma unroll 1
  for (unsigned spins = 0; spins < SPIN_CAP; ++spins) {
    ldm3(a, rs, v0, v1, v2, miss);
    miss &= untagged(a[0], tg) | untagged(a[1], tg) | (untagged(a[2], tg) & care2);
    if (miss == 0ull) break;
  }
}
#define LDM(i, m) "s_mov_b64 exec, %" #m "\n\tbuffer_load_dwordx4 %" #i ", %7, %6, %5 offen sc1\n\ts_add_u32 %5, %5, 0x1000\n\t"
FI void ldm4(v4u& a0, v4u& a1, v4u& a2, v4u& a3, const v4i& rs, unsigned v0, unsigned so, unsigned long long m0, unsigned long long m1,
             unsigned long long m2, unsigned long long m3) {
  unsigned long long keep;
  asm volatile("s_mov_b64 %4, exec\n\ts_nop 3\n\t" LDM(0, 8) LDM(1, 9) LDM(2, 10) LDM(3, 11) "s_mov_b64 exec, %4"
               : "+v"(a0), "+v"(a1), "+v"(a2), "+v"(a3), "=&s"(keep), "+s"(so)
               : "s"(rs), "v"(v0), "s"(m0), "s"(m1), "s"(m2), "s"(m3)
               : "memory", "scc");
}
FI void pin4(v4u& a0, v4u& a1, v4u& a2, v4u& a3) { asm volatile("s_waitcnt vmcnt(0)" : "+v"(a0), "+v"(a1), "+v"(a2), "+v"(a3) :: "memory"); }
FI void lds_put(unsigned addr, unsigned v) { asm volatile("s_waitcnt lgkmcnt(0)\n\tds_write_b32 %0, %1" ::"v"(addr), "v"(v) : "memory"); }
FI unsigned lds_get(unsigned addr) {
  unsigned v;
  asm volatile("ds_read_b32 %0, %1\n\ts_waitcnt lgkmcnt(0)" : "=v"(v) : "v"(addr) : "memory");
  return __builtin_amdgcn_readfirstlane(v);
}
template <int SLOT> FI void read_k(Tile& k, unsigned addr) {
  unsigned tmp;
  asm volatile(
      "ds_read_b128 %0, %5 offset:%6\n\t"
      "v_xor_b32 %4, 16, %5\n\t"
      "ds_read_b128 %1, %4 offset:%6\n\t"
      "v_xor_b32 %4, 32, %5\n\t"
      "ds_read_b128 %2, %4 offset:%6\n\t"
      "v_xor_b32 %4, 48, %5\n\t"
      "ds_read_b128 %3, %4 offset:%6\n\t"
      "s_waitcnt lgkmcnt(0)"
      : "=&v"(k.r[0]), "=&v"(k.r[1]), "=&v"(k.r[2]), "=&v"(k.r[3]), "=&v"(tmp)
      : "v"(addr), "i"(SLOT * 4096)
      : "memory");
}
FI f4 qk(const Tile& k, const Tile& q) {
  f4 s;
  asm volatile(
      "v_mfma_f32_16x16x32_bf16 %0, %1, %5, 0\n\t"
      "v_mfma_f32_16x16x32_bf16 %0, %2, %6, %0\n\t"
      "v_mfma_f32_16x16x32_bf16 %0, %3, %7, %0\n\t"
      "v_mfma_f32_16x16x32_bf16 %0, %4, %8, %0\n\t"
      "s_nop 7\n\ts_nop 1"
      : "=&v"(s)
      : "v"(k.r[0]), "v"(k.r[1]), "v"(k.r[2]), "v"(k.r[3]), "v"(q.r[0]), "v"(q.r[1]), "v"(q.r[2]), "v"(q.r[3]));
  return s;
}
FI void load_b64(v2u& d, unsigned voff, const v4i& rs, unsigned soff) {
  asm volatile("s_nop 4\n\tbuffer_load_dwordx2 %0, %1, %2, %3 offen nt" : "=v"(d) : "v"(voff), "s"(rs), "s"(soff) : "memory");
}
template <int N> FI void wait_wq(v2u (&q)[12]) {
  asm volatile("s_waitcnt vmcnt(%12)"
               : "+v"(q[0]), "+v"(q[1]), "+v"(q[2]), "+v"(q[3]), "+v"(q[4]), "+v"(q[5]), "+v"(q[6]), "+v"(q[7]), "+v"(q[8]), "+v"(q[9]),
                 "+v"(q[10]), "+v"(q[11])
               : "i"(N) : "memory");
}

struct Soft { f4 acc; float mc, l; };

template <bool LATER> FI v2u softmax_tile(Soft& st, const f4& s, float* slot, unsigned& moved, unsigned bit) {
  f4 t;
#pragma unroll
  for (int r = 0; r < 4; ++r) t[r] = __builtin_fmaf(s[r], QSCALE, -st.mc);
  const float tm = fmaxf(fmaxf(t[0], t[1]), fmaxf(t[2], t[3]));
  if (__builtin_amdgcn_ballot_w64(tm > 0.f) != 0ull) {
    const float sm = fmaxf(fmaxf(s[0], s[1]), fmaxf(s[2], s[3])) * QSCALE;
    const float mn = fmaxf(st.mc, xrow_max(sm));
    const float al = ex2(st.mc - mn);
    st.mc = mn;
    st.l *= al;
    if constexpr (LATER) { *slot = al; moved |= bit; } else st.acc *= al;
#pragma unroll
    for (int r = 0; r < 4; ++r) t[r] = __builtin_fmaf(s[r], QSCALE, -mn);
  }
  f4 p;
#pragma unroll
  for (int r = 0; r < 4; ++r) p[r] = ex2(t[r]);
  st.l += (p[0] + p[1]) + (p[2] + p[3]);
  v2u pp = {pk_bf(p[0], p[1]), pk_bf(p[2], p[3])};
  return pp;
}
#define PV_MFMAS \
  "s_nop 1\n\t" \
  "v_mfma_f32_16x16x32_bf16 %0, %1, %5, %0\n\t" \
  "v_mfma_f32_16x16x32_bf16 %0, %2, %6, %0\n\t" \
  "v_mfma_f32_16x16x32_bf16 %0, %3, %7, %0\n\t" \
  "v_mfma_f32_16x16x32_bf16 %0, %4, %8, %0\n\t" \
  "s_nop 7\n\ts_nop 1"
template <bool A> FI void pv_tile(Soft& st, const v2u& p, const Tile& v, const Masks& k) {
  const v4u b0 = diag(dup_lo(p[0]), k), b1 = diag(dup_hi(p[0]), k), b2 = diag(dup_lo(p[1]), k), b3 = diag(dup_hi(p[1]), k);
  if constexpr (A)
    asm volatile(PV_MFMAS : "+v"(st.acc) : "a"(v.r[0]), "a"(v.r[1]), "a"(v.r[2]), "a"(v.r[3]), "v"(b0), "v"(b1), "v"(b2), "v"(b3));
  else
    asm volatile(PV_MFMAS : "+v"(st.acc) : "v"(v.r[0]), "v"(v.r[1]), "v"(v.r[2]), "v"(v.r[3]), "v"(b0), "v"(b1), "v"(b2), "v"(b3));
}

constexpr bool vr_in_a(int i) { return i < 7; }

constexpr int imin(int a, int b) { return a < b ? a : b; }
constexpr int imax(int a, int b) { return a > b ? a : b; }

constexpr int AUX_SC1 = 16, AUX_NT = 2;

__global__ void __launch_bounds__(256, 1)
fused_decode_kernel(const unsigned short* __restrict__ xin, const unsigned char* __restrict__ kc, const unsigned char* __restrict__ vc,
                    const unsigned char* __restrict__ wq, const unsigned char* __restrict__ wo, const unsigned char* __restrict__ wg,
                    const unsigned char* __restrict__ wu, const unsigned char* __restrict__ wd, unsigned short* __restrict__ out,
                    unsigned char* ws) {
  __shared__ __attribute__((aligned(16))) unsigned char smem[L_BYTES];
  const int b = blockIdx.x;
  const int w = __builtin_amdgcn_readfirstlane(threadIdx.x >> 6);
  const int head = b >> 4, split = b & 15;
  const int myr = b - (NWG - NRED);
  const unsigned lbase = (unsigned)(size_t)smem;

  const v4i rsK = mk_rsrc(kc + ((size_t)head << 24), 1u << 24);
  const v4i rsV = mk_rsrc(vc + ((size_t)head << 24), 1u << 24);
  const v4i rsQ = mk_rsrc(wq, DM * NQ * 2);
  const v4i rsC = mk_rsrc(ws, WS_BYTES);
  const __amdgpu_buffer_rsrc_t rsW = __builtin_amdgcn_make_buffer_rsrc(ws, 0, WS_BYTES, 0x00020000);

  const unsigned m0base = lbase + L_KS + w * (NL * 4096);
  const unsigned soffw = split * 16384 + w * 4096;
  auto toff = [&](unsigned t) { return soffw + (((t + head * 16u) & 63u) << 18); };
  int lane, g, c, tid;
  Masks mk;
  unsigned voffV, voffK, addrK;
  auto relane = [&]() {
    const Ln L = make_ln();
    lane = L.lane; g = L.g; c = L.c; mk = L.mk; tid = w * 64 + lane;
    voffV = lane * 16;
    voffK = g * 256 + ((c ^ g) << 4);
    const unsigned kap = 4 * (c & 3) + (c >> 2);
    addrK = m0base + kap * 256 + (((4 * g) ^ kap) << 4);
  };
  relane();

  Tile KR[NR], VR[NR], VL[NL];

  auto now8 = [&]() { return (unsigned)__builtin_amdgcn_s_memrealtime() << 8; };
  unsigned t_go = 0, due = 0;
  auto gate = [&](unsigned pace) {
    due += pace;
    while ((int)(now8() - due) < 0) __builtin_amdgcn_s_sleep(1);
  };
  auto send_k = [&](int slot, unsigned t) { issue_k(m0base + slot * 4096, voffK, rsK, toff(t)); };

  const int qs = (b & 7) * 32 + (b >> 3);
  v2u wqf[12];
  auto issue_wq = [&]() {
    const unsigned voffQ = (192u * w + (4 * g + (c >> 2)) * 12u) * (NQ * 2) + (unsigned)(qs * 16 + 4 * (c & 3)) * 2u;
    sfor<0, 12>([&](auto I) {
      load_b64(wqf[I.value], voffQ, rsQ, I.value * NQ * 2);
    });
  };

  auto xpiece = [&](int np, int k) { const unsigned j = np * w + lane + 64 * k; return j >= 144u ? j - 144u : j; };
  auto layernorm = [&](v2u (&px)[3], int np) {
    const bool act2 = lane < 16;
    auto up = [](unsigned pr) { return (f2){bf_lo(pr), bf_hi(pr)}; };
    const float x0 = bf_lo(__builtin_amdgcn_readfirstlane(px[0][0]));
    f2 s = {0.f, 0.f}, q = {0.f, 0.f};
#pragma unroll
    for (int k = 0; k < 3; ++k) {
#pragma unroll
      for (int i = 0; i < 2; ++i) {
        f2 a = up(px[k][i]) - x0;
        if (k == 2) a = act2 ? a : (f2){0.f, 0.f};
        s += a;
        q = __builtin_elementwise_fma(a, a, q);
      }
    }
    float sq[2] = {s[0] + s[1], q[0] + q[1]};
    row_sums(sq);
    xrow_sums2(sq[0], sq[1]);
    const float md = sq[0] * (1.f / DM);
    const float mean = x0 + md;
    const float var = fmaxf(__builtin_fmaf(-md, md, sq[1] * (1.f / DM)), 0.f);
    const float mb = rbf(mean);
    const float den = rbf(__builtin_amdgcn_sqrtf(rbf(rbf(var) + 1e-5f)));
    const float rc = __builtin_amdgcn_rcpf(den);
    auto dv = [&](unsigned pr) {
      const unsigned a2 = pk_bf(bf_lo(pr) - mb, bf_hi(pr) - mb);
      const f2 a = {bf_lo(a2), bf_hi(a2)};
      const f2 q0 = a * rc;
      const f2 q1 = __builtin_elementwise_fma(__builtin_elementwise_fma(-q0, (f2){den, den}, a), (f2){rc, rc}, q0);
      return pk_bf(q1[0], q1[1]);
    };
    v2u o = {dv(px[0][0]), dv(px[0][1])};
    if (lane < np) *(v2u*)(smem + L_XB + (np * w + lane) * 8) = o;
  };

  auto q_gemv = [&]() {
    if (w < 3) {
      const v2u* xp = (const v2u*)(smem + L_XB + (192 * w + (4 * g + (c >> 2)) * 12) * 2);
      const v2u xa = xp[0], xb = xp[1], xc = xp[2];
      const unsigned xs[6] = {xa[0], xa[1], xb[0], xb[1], xc[0], xc[1]};
      const unsigned ma = mk.m0 | mk.m2, mb = mk.m1 | mk.m3;
      f4 d;
#pragma unroll
      for (int n = 0; n < 6; ++n) {
        const unsigned lo = dup_lo(xs[n]), hi = dup_hi(xs[n]);
        const v4u bb = {lo & ma, lo & mb, hi & ma, hi & mb};
        const v4u aa = {wqf[2 * n][0], wqf[2 * n][1], wqf[2 * n + 1][0], wqf[2 * n + 1][1]};
        if (n == 0) d = mfma_first(aa, bb); else mfma_acc(d, aa, bb);
      }
      mfma_fence(d);
      if ((c >> 2) == g) {
        float* rp = (float*)(smem + L_REDA) + (w * 4 + g) * 16 + (c & 3);
#pragma unroll
        for (int r = 0; r < 4; ++r) rp[4 * r] = d[r];
      }
    }
  };
  auto q_publish = [&](unsigned tag) {
    const f2* rp = (const f2*)(smem + L_REDA) + (lane & 7);
    f2 a[12];
#pragma unroll
    for (int i = 0; i < 12; ++i) a[i] = rp[8 * i];
    const f2 t = (((a[0] + a[1]) + (a[2] + a[3])) + ((a[4] + a[5]) + (a[6] + a[7]))) + ((a[8] + a[9]) + (a[10] + a[11]));
    v2u gr = {pk_bf(t[0], t[1]), tag};
    __builtin_amdgcn_raw_buffer_store_b64(gr, rsW, lane < 8 ? WS_QG + (qs * 8 + lane) * 8 : OOB, 0, AUX_SC1);
  };
  auto q_keep = [&](const v4u& v) { v2u o = {v[0], v[2]}; *(v2u*)(smem + L_QB + lane * 8) = o; };

  unsigned long long pc;
  unsigned code_len;
  asm volatile("s_getpc_b64 %0\n.Lfd_code_from:\n\ts_mov_b32 %1, .Lfd_code_to-.Lfd_code_from" : "=s"(pc), "=s"(code_len));
  auto warm_code = [&]() { touch(lbase + L_PAD, lane * 64, mk_rsrc((const void*)pc, code_len), (b >> 3) * 4096); };
  auto put_tb = [&](unsigned tag, unsigned t) {
    if (b >= NWG - 8 && lane == 0) { v2u tb = {t, tag}; __builtin_amdgcn_raw_buffer_store_b64(tb, rsW, WS_TB + (b & 7) * 8, 0, AUX_SC1); }
  };
  auto keep_tb = [&](const v4u& tb) {
    int d = max((int)(tb[0] - t_go), (int)(tb[2] - t_go));
    d = max(d, __builtin_amdgcn_mov_dpp(d, 0xB1, 0xf, 0xf, true));
    d = max(d, __builtin_amdgcn_mov_dpp(d, 0x4E, 0xf, 0xf, true));
    lds_put(lbase + L_FLAG + 28, t_go + d);
  };

  unsigned tagbase;
  auto TAG = [&](unsigned ph) { return 0x80000000u | ((tagbase + ph) & 0x7fffffffu); };
  auto lds_wait = [&](int word) { for (unsigned i = 0; i < 8u * SPIN_CAP && lds_get(lbase + L_FLAG + 4 * word) == 0u; ++i) __builtin_amdgcn_s_sleep(1); };
  auto head_step = [&](auto Qi) {
    constexpr int q = Qi.value;
    if constexpr (q == D) { if (w != 3) { wait_vm<4 * (D - 1) + 12>(); t_go = now8(); } due = t_go + PACE_OFF1 * 256; }
    if constexpr (q >= D) wait_vm<4 * (D - 1) + (q - D < D ? 12 : 0)>();
    gate(PACE_R);
    if constexpr (q < NR) send_k(q, q);
    else if constexpr (q < 2 * NR) { issue_v<vr_in_a(q - NR) ? 0 : 1>(VR[q - NR], voffV, rsV, toff(q - NR)); read_k<q - NR>(KR[q - NR], addrK); }
    else send_k(q - 2 * NR, q - NR);
  };
  {
    v2u px[3];
    unsigned ep;
    send_k(0, 0);
    __builtin_amdgcn_sched_barrier(0);
    {
      const v4u z = {0u, 0u, 0u, 0u};
      if (tid < 144) __builtin_amdgcn_raw_buffer_store_b128(z, rsW, WS_PA + (head * 16 + tid / 9) * PA_STRIDE + split * PA_MSG + (tid % 9) * 16, 0, AUX_SC1);
      __builtin_amdgcn_raw_buffer_store_b128(z, rsW, WS_PB + (tid >> 3) * PB_STRIDE + b * 128 + (tid & 7) * 16, 0, AUX_SC1);
      if (tid < 32) __builtin_amdgcn_raw_buffer_store_b128(z, rsW, WS_PB + (32 + (tid >> 3)) * PB_STRIDE + b * 128 + (tid & 7) * 16, 0, AUX_SC1);
      if (myr >= 0 && tid < 4 * XREP) __builtin_amdgcn_raw_buffer_store_b128(z, rsW, WS_XG + (tid >> 2) * XG_STRIDE + myr * 64 + (tid & 3) * 16, 0, AUX_SC1);
      asm volatile("" ::: "memory");
    }
    const v4i rsX = mk_rsrc(xin, DM * 2);
    const v4i rsE = mk_rsrc(ws, 64);
    asm volatile("buffer_load_dwordx2 %0, %4, %7, 0 offen\n\t"
                 "buffer_load_dwordx2 %1, %5, %7, 0 offen\n\t"
                 "buffer_load_dwordx2 %2, %6, %7, 0 offen\n\t"
                 "buffer_load_dword %3, off, %8, 0 sc1"
                 : "=&v"(px[0]), "=&v"(px[1]), "=&v"(px[2]), "=&v"(ep)
                 : "v"(xpiece(48, 0) * 8), "v"(xpiece(48, 1) * 8), "v"(lane < 16 ? xpiece(48, 2) * 8 : OOB), "s"(rsX), "s"(rsE) : "memory");
    sfor<1, D>([&](auto I) { send_k(I.value, I.value); });
    issue_wq();
    if (w == 2) warm_code();
    if (tid < 8) *(unsigned*)(smem + L_FLAG + 4 * tid) = 0u;
    wg_barrier();
    if (w == 3) {
      asm volatile("s_waitcnt vmcnt(%4)" : "+v"(px[0]), "+v"(px[1]), "+v"(px[2]), "+v"(ep) : "i"(4 * (D - 1) + 12) : "memory");
      tagbase = __builtin_amdgcn_readfirstlane(ep);
      t_go = now8();
      put_tb(TAG(12), t_go);
#pragma unroll
      for (int k = 0; k < 3; ++k) if (((lane + 64 * k) >> 2) == myr) *(v2u*)(smem + L_XR + (lane & 3) * 8) = px[k];
    }
    sfor<D, QG_AT>(head_step);
    wait_wq<4 * (QG_AT - D)>(wqf);
    asm volatile("" : "+v"(ep), "+v"(px[0]), "+v"(px[1]), "+v"(px[2]));
    tagbase = __builtin_amdgcn_readfirstlane(ep);
    if (w < 3) layernorm(px, 48);
  }
  if (w < 3) {
    q_gemv();
    lds_put(lbase + L_FLAG + 4 + 4 * w, 1u);
  }
  v4u ride;
  def_v(ride);
  if (w == 3) ride_load(ride, rsC, WS_TB + (lane & 3) * 16);
  sfor<QG_AT, QG_AT + D + 1>(head_step);
  if (w == 3) {
    v4u a[3];
    a[0] = ride_take(ride);
    if (untagged(a[0], TAG(12)) != 0ull) poll3(a, rsC, WS_TB + (lane & 3) * 16, WS_TB + (lane & 3) * 16, OOB, TAG(12), 0ull);
    keep_tb(a[0]);
    lds_put(lbase + L_FLAG + 20, 1u);
    lds_wait(1); lds_wait(2); lds_wait(3);
    q_publish(TAG(1));
  }
  lds_wait(5);
  due += lds_get(lbase + L_FLAG + 28) + TB_DELTA * 256 - t_go;
  sfor<QG_AT + D + 1, 2 * NR - 2>(head_step);
  if (w == 3) ride_load(ride, rsC, WS_QG + head * 1024 + lane * 16);
  sfor<2 * NR - 2, 2 * NR + D>(head_step);
  if (w == 3) {
    v4u a[3];
    a[0] = ride_take(ride);
    if (untagged(a[0], TAG(1)) != 0ull) poll3(a, rsC, WS_QG + head * 1024 + lane * 16, WS_QG + head * 1024 + lane * 16, OOB, TAG(1), 0ull);
    q_keep(a[0]);
    lds_put(lbase + L_FLAG + 16, 1u);
  }
  lds_wait(4);

  Tile Q;
  Soft st, st2;
  unsigned moved;
  auto load_q = [&]() {
    const v4u* qp = (const v4u*)(smem + L_QB + (c >> 3) * 256 + g * 64);
#pragma unroll
    for (int j = 0; j < 4; ++j) Q.r[j] = qp[j];
    st.acc = (f4){0.f, 0.f, 0.f, 0.f};
    st.mc = -__builtin_inff();
    st.l = 0.f;
    st2 = st;
  };
  auto merge_st2 = [&]() {
    const float m = fmaxf(st.mc, st2.mc), a1 = ex2(st.mc - m), a2 = ex2(st2.mc - m);
    st.acc = st.acc * a1 + st2.acc * a2;
    st.l = st.l * a1 + st2.l * a2;
    st.mc = m;
  };
  auto tile_r = [&](Soft& s, auto I) { pv_tile<vr_in_a(I.value)>(s, softmax_tile<false>(s, qk(KR[I.value], Q), nullptr, moved, 0u), VR[I.value], mk); };
  auto tile_l = [&](Soft& s, auto I) {
    Tile kt;
    read_k<I.value>(kt, addrK);
    pv_tile<true>(s, softmax_tile<false>(s, qk(kt, Q), nullptr, moved, 0u), VL[I.value], mk);
  };
  WoFrag wo1, wo2;
  auto load_wo = [&](WoFrag& x) {
    const __amdgpu_buffer_rsrc_t r = __builtin_amdgcn_make_buffer_rsrc((void*)wo, 0, DM * NQ * 2, 0x00020000);
    const unsigned p = (b * 16 + 4 * g) * (DM * 2);
#pragma unroll
    for (int n = 0; n < 4; ++n) x.f[n] = __builtin_amdgcn_raw_buffer_load_b128(r, p + n * (DM * 2) + 256 * w + 16 * c, 0, AUX_NT);
#pragma unroll
    for (int n = 0; n < 2; ++n) x.c[n] = __builtin_amdgcn_raw_buffer_load_b128(r, p + (2 * n + (c >> 3)) * (DM * 2) + 1024 + 16 * (c & 7), 0, AUX_NT);
    asm volatile("" ::: "memory");
  };
  auto block = [&](auto NTc, auto PARKc, float* alp, unsigned t0, unsigned pace, bool more, auto&& side) {
    constexpr int NT = NTc.value;
    constexpr bool PARK = PARKc.value;
    v2u pend[NT];
    moved = 0u;
    sfor<0, 2 * NT>([&](auto Sc) {
      constexpr int s = Sc.value, q = s + D;
      if constexpr (s + D - 1 < 2 * NT) wait_vm<4 * (D - 1)>();
      else { if (more) wait_vm<4 * (D - 1)>(); else wait_vm<4 * (2 * NT - 1 - s)>(); }
      if constexpr (s < NT) {
        Tile kt;
        read_k<s % D>(kt, addrK);
        if constexpr (q < NT) { gate(pace); send_k(q % D, t0 + q); }
        else { gate(pace); issue_v<2>(VL[(q - NT) % D], voffV, rsV, toff(t0 + q - NT)); }
        const v2u pp = softmax_tile<true>(st, qk(kt, Q), alp + s * 64, moved, 1u << s);
        pend[s][0] = park<PARK>(pp[0]); pend[s][1] = park<PARK>(pp[1]);
      } else {
        constexpr int j = s - NT;
        if (moved & (1u << j)) st.acc *= alp[j * 64];
        const v2u pp = {unpark<PARK>(pend[j][0]), unpark<PARK>(pend[j][1])};
        pv_tile<true>(st, pp, VL[j % D], mk);
        if constexpr (q < 2 * NT) { gate(pace); issue_v<2>(VL[(q - NT) % D], voffV, rsV, toff(t0 + q - NT)); }
        else if (more) { gate(pace); send_k(q - 2 * NT, t0 + NT + q - 2 * NT); }
      }
      side(Sc);
    });
  };
  using I0 = std::integral_constant<int, 0>;
  using I1 = std::integral_constant<int, 1>;

  auto warm = [&](int wi) {
    const v4i rsG = mk_rsrc(wg, DM * DFF * 2), rsU = mk_rsrc(wu, DM * DFF * 2), rsD = mk_rsrc(wd, DM * DFF * 2);
    const unsigned q = (b >> 3) * 1536u + tid + 256 * wi, r = q / 72u;
    const unsigned vg = r * (DFF * 2) + (q - r * 72u) * 16u;
    touch16(lbase + L_PAD, vg, rsG, (b & 7) * 1152u);
    touch16(lbase + L_PAD, vg, rsU, (b & 7) * 1152u);
    touch16(lbase + L_PAD, q < 41472u ? q * 16u : OOB, rsD, (b & 7) * (576u * DM * 2));
  };
  auto chain = [&](auto LAYER, const WoFrag& wf) {
    constexpr int layer = LAYER.value;
    relane();
    const unsigned ph = 2 + 5 * layer;

    const int mu = (b & 7) * 18 + (b >> 3);
    constexpr int GW = layer == 0 ? GATE_WIN : 27;
    v4u wb[GW];
    const unsigned char* pgu;
    unsigned odn, odc;
    auto bases = [&]() {
      pgu = (((c >> 2) & 1) ? wu : wg) + (size_t)(144 * w + (2 * g + (c >> 3)) * 18) * (DFF * 2) + 64 * mu + 16 * (c & 3);
      odn = (32 * mu + 8 * w + 2 * g) * (DM * 2) + 16 * c;
      odc = (32 * mu + 8 * w + 2 * g + (c >> 3)) * (DM * 2) + 1024 + 16 * (c & 7);
    };
    const __amdgpu_buffer_rsrc_t rsDn = __builtin_amdgcn_make_buffer_rsrc((void*)wd, 0, DM * DFF * 2, 0x00020000);
    auto frag = [&](auto F) {
      constexpr int f = F.value;
      if constexpr (f < 18) wb[f % GW] = *(const v4u*)(pgu + (size_t)f * (DFF * 2));
      else if constexpr (f < 26) wb[f % GW] = __builtin_amdgcn_raw_buffer_load_b128(rsDn, odn + ((f - 18) & 1) * (DM * 2) + ((f - 18) >> 1) * 256, 0, 0);
      else if constexpr (f == 26) wb[f % GW] = __builtin_amdgcn_raw_buffer_load_b128(rsDn, odc, 0, 0);
    };
    {
      const float lt = xrow_sum(st.l);
      float* cp = (float*)(smem + L_REDB) + w * (NTA * 64);
#pragma unroll
      for (int r = 0; r < 4; ++r) cp[(c >> 3) * 128 + 32 * g + 8 * r + (c & 7)] = st.acc[r];
      if ((lane & 0x37) == 0) { f2 ml = {st.mc, lt}; *(f2*)(cp + 256 + 2 * (c >> 3)) = ml; }
    }
    wg_barrier();
    {
      const float* cp = (const float*)(smem + L_REDB);
      const int h = tid >> 7;
      f2 ml[4];
#pragma unroll
      for (int i = 0; i < 4; ++i) ml[i] = *(const f2*)(cp + i * (NTA * 64) + 256 + 2 * h);
      const float M = fmaxf(fmaxf(ml[0][0], ml[1][0]), fmaxf(ml[2][0], ml[3][0]));
      float O = 0.f, L = 0.f;
#pragma unroll
      for (int i = 0; i < 4; ++i) { const float sc = ex2(ml[i][0] - M); O += sc * cp[i * (NTA * 64) + tid]; L += sc * ml[i][1]; }
      const unsigned tg = TAG(ph);
      const unsigned pa = WS_PA + (head * 16 + (tid >> 4)) * PA_STRIDE + split * PA_MSG;
      v2u gr = {__float_as_uint(O), tg};
      __builtin_amdgcn_raw_buffer_store_b64(gr, rsW, pa + c * 8, 0, AUX_SC1);
      if (c == 0) {
        v4u g2 = {__float_as_uint(M), tg, __float_as_uint(L), tg};
        __builtin_amdgcn_raw_buffer_store_b128(g2, rsW, pa + 128, 0, AUX_SC1);
      }
    }
    relane();
    {
      const unsigned tg = TAG(ph);
      const unsigned base = WS_PA + b * PA_STRIDE + c * PA_MSG;
      v4u a[3];
      const unsigned tb = TAG(ph + 1);
      const bool hp = (c >> 3) != 0, odd = (c & 1) != 0;
      const unsigned lo_ = WS_PB + b * 128 + (odd ? 64 : 0) + (c & 6) * 8;
      unsigned ob1 = lo_ + (8 * w + 2 * g + (hp ? 1 : 0)) * PB_STRIDE, ob2 = g == (w >> 1) && !hp ? lo_ + (32 + w) * PB_STRIDE : OOB;
      pin(mk); pin(ob1); pin(ob2);
      __builtin_amdgcn_s_sleep(6);
      poll3(a, rsC, base + 32 * g, base + 32 * g + 16, base + 128, tg, ~0ull);
      const float ms = __uint_as_float(a[2][0]);
      const float wgt = ex2(ms - row_max(ms));
      float o[5] = {wgt * __uint_as_float(a[0][0]), wgt * __uint_as_float(a[0][2]), wgt * __uint_as_float(a[1][0]), wgt * __uint_as_float(a[1][2]),
                    wgt * __uint_as_float(a[2][2])};
      row_sums(o);
      const float inv = frcp(o[4]);
#pragma unroll
      for (int n = 0; n < 4; ++n) o[n] *= inv;
      auto put_line = [&](float lo, float hi, unsigned off) {
        const float lon = dpp<0xB1>(lo), hin = dpp<0xB1>(hi);
        v4u gr = {__float_as_uint(odd ? hin : lo), tb, __float_as_uint(odd ? hi : lon), tb};
        __builtin_amdgcn_raw_buffer_store_b128(gr, rsW, off, 0, AUX_SC1);
      };
      const float oa = hp ? o[1] : o[0], obv = hp ? o[3] : o[2];
      f4 e = mfma_first(wf.c[0], diag(pk_bf(oa, oa), mk));
      mfma_acc(e, wf.c[1], diag(pk_bf(obv, obv), mk));
      f4 d = mfma_first(wf.f[0], diag(pk_bf(o[0], o[0]), mk));
#pragma unroll
      for (int n = 1; n < 4; ++n) mfma_acc(d, wf.f[n], diag(pk_bf(o[n], o[n]), mk));
      mfma_fence2(d, e);
      put_line(hp ? d[2] : d[0], hp ? d[3] : d[1], ob1);
      {
        const float lo = (w & 1) ? e[2] : e[0], hi = (w & 1) ? e[3] : e[1];
        float f[2] = {(g >> 1) == (hp ? 1 : 0) ? lo : 0.f, (g >> 1) == (hp ? 1 : 0) ? hi : 0.f};
#pragma unroll
        for (int r = 0; r < 2; ++r) {
          const float t = f[r] + dpp<0x128>(f[r]);
          auto sw = __builtin_amdgcn_permlane32_swap(__float_as_uint(t), __float_as_uint(t), false, false);
          f[r] = __uint_as_float(sw[0]) + __uint_as_float(sw[1]);
        }
        put_line(f[0], f[1], ob2);
      }
    }
    relane();
    if (layer == 0 && myr < 0) {
#pragma unroll
      for (int i = WARM1; i < 6; ++i) warm(i);
      if (w == 2) warm_code();
    }
    auto reduce_cols = [&](auto NPc, unsigned tg, auto FINAL, unsigned tgx) {
      constexpr int NP = NPc.value, NK = (NP * 8 + 255) / 256;
      v4u a[8];
      unsigned long long miss[8];
#pragma unroll
      for (int k = 0; k < 8; ++k) { a[k] = (v4u){0u, 0u, 0u, 0u}; miss[k] = __builtin_amdgcn_ballot_w64(tid + 256 * k < NP * 8); }
      const unsigned base = WS_PB + myr * PB_STRIDE;
      v2u xo = *(const v2u*)(smem + L_XR + (tid & 3) * 8);
      unsigned ox = FINAL.value ? (tid < 4 ? 32 * myr + 8 * tid : OOB) : (tid < 4 * XREP ? WS_XG + (tid >> 2) * XG_STRIDE + myr * 64 + (tid & 3) * 16 : OOB);
      unsigned x0 = xo[0], x1 = xo[1], ra = L_REDA + (tid & 3) * 16;
      pin(x0); pin(x1); pin(ox); pin(ra);
      if constexpr (NP == NWG) __builtin_amdgcn_s_sleep(12);
#pragma unroll 1
      for (unsigned spins = 0; spins < SPIN_CAP; ++spins) {
        ldm4(a[0], a[1], a[2], a[3], rsC, tid * 16, base, miss[0], miss[1], miss[2], miss[3]);
        if ((miss[4] | miss[5] | miss[6] | miss[7]) != 0ull) ldm4(a[4], a[5], a[6], a[7], rsC, tid * 16, base + 16384, miss[4], miss[5], miss[6], miss[7]);
        pin4(a[4], a[5], a[6], a[7]);
        pin4(a[0], a[1], a[2], a[3]);
        unsigned long long any = 0ull;
#pragma unroll
        for (int k = 0; k < NK; ++k) { miss[k] &= untagged(a[k], tg); any |= miss[k]; }
        if (any == 0ull) break;
      }
      float s0 = __uint_as_float(a[0][0]), s1 = __uint_as_float(a[0][2]);
#pragma unroll
      for (int k = 1; k < NK; ++k) { s0 += __uint_as_float(a[k][0]); s1 += __uint_as_float(a[k][2]); }
      s0 += dpp<0x128>(s0); s1 += dpp<0x128>(s1);
      xrow_sums2(s0, s1);
      if (lane < 8) ((f2*)(smem + L_REDA))[w * 8 + lane] = (f2){s0, s1};
      wg_barrier();
      if (w == 0) {
        const f4* rp = (const f4*)(smem + ra);
        const f4 t = (rp[0] + rp[4]) + (rp[8] + rp[12]);
        const unsigned t0 = pk_bf(t[0], t[1]), t1 = pk_bf(t[2], t[3]);
        const v2u xn = {pk_bf(bf_lo(x0) + bf_lo(t0), bf_hi(x0) + bf_hi(t0)), pk_bf(bf_lo(x1) + bf_lo(t1), bf_hi(x1) + bf_hi(t1))};
        if constexpr (FINAL.value) {
          __builtin_amdgcn_raw_buffer_store_b64(xn, __builtin_amdgcn_make_buffer_rsrc(out, 0, DM * 2, 0x00020000), ox, 0, 0);
        } else {
          const v4u gx = {xn[0], tgx, xn[1], tgx};
          __builtin_amdgcn_raw_buffer_store_b128(gx, rsW, ox, 0, AUX_SC1);
          if (tid < 4) *(v2u*)(smem + L_XR + tid * 8) = xn;
        }
      }
    };
    auto gather_x_ln = [&](unsigned tg, int np) {
      v4u a[3];
      const unsigned xb = WS_XG + (b % XREP) * XG_STRIDE;
      poll3(a, rsC, xb + xpiece(np, 0) * 16, xb + xpiece(np, 1) * 16, lane < 16 ? xb + xpiece(np, 2) * 16 : OOB, tg, 0xffffull);
      v2u px[3];
#pragma unroll
      for (int k = 0; k < 3; ++k) { px[k][0] = a[k][0]; px[k][1] = a[k][2]; }
      layernorm(px, np);
    };

    if (b < NMLP) {
      bases();
      if constexpr (layer == 0) wait_vm<0>();
      sfor<0, GW>(frag);
      unsigned xoff = L_XB + (144 * w + (2 * g + (c >> 3)) * 18) * 2;
      if constexpr (layer != 0) { pin(mk); pin(xoff); }
      gather_x_ln(TAG(ph + 2), 36);
      if constexpr (layer == 0) { relane(); bases(); xoff = L_XB + (144 * w + (2 * g + (c >> 3)) * 18) * 2; }
      f4 d;
      {
        const unsigned* xp = (const unsigned*)(smem + xoff);
        unsigned xs[9];
        constexpr int XA = layer == 0 ? 4 : 9;
#pragma unroll
        for (int n = 0; n < XA; ++n) xs[n] = xp[n];
        sfor<0, 9>([&](auto Nn) {
          constexpr int n = Nn.value;
          const v4u b0 = diag(dup_lo(xs[n]), mk), b1 = diag(dup_hi(xs[n]), mk);
          if constexpr (n + XA < 9) xs[n + XA] = xp[n + XA];
          if constexpr (n == 0) d = mfma_first2(wb[0], b0, wb[1], b1); else mfma_acc2(d, wb[(2 * n) % GW], b0, wb[(2 * n + 1) % GW], b1);
          if constexpr (layer == 0) __builtin_amdgcn_sched_barrier(0);
          frag(std::integral_constant<int, 2 * n + GW>{});
          frag(std::integral_constant<int, 2 * n + 1 + GW>{});
          if constexpr (layer == 0) __builtin_amdgcn_sched_barrier(0);
        });
      }
      const bool hp = (c >> 3) != 0;
      unsigned ra = L_REDA + ((w * 4 + g) * 32 + (c & 7)) * 4, rb = L_REDA + (4 * w + g + 32 * (c & 7) + 16 * (c >> 3)) * 8;
      unsigned rc_ = L_REDB + (w * 512 + 32 * g + (c & 7)) * 4, rd = L_REDB + (2048 + w * 128 + 32 * g + (c & 7)) * 4;
      unsigned o1 = WS_PB + (tid >> 3) * PB_STRIDE + mu * 128 + (tid & 7) * 16, o2 = lane < 8 ? WS_PB + (32 + w) * PB_STRIDE + mu * 128 + lane * 16 : OOB;
      pin(ra); pin(rb); pin(rc_); pin(rd); pin(o1); pin(o2);
      mfma_fence(d);
      if ((g >> 1) == (c >> 3)) {
        float* rp = (float*)(smem + ra);
#pragma unroll
        for (int r = 0; r < 4; ++r) rp[8 * r] = d[r];
      }
      wg_barrier();
      unsigned hd0, hd1;
      {
        const f2 v = *(const f2*)(smem + rb);
        float t[2] = {v[0], v[1]};
#pragma unroll
        for (int i = 0; i < 2; ++i) t[i] += dpp<0xB1>(t[i]);
#pragma unroll
        for (int i = 0; i < 2; ++i) t[i] += dpp<0x4E>(t[i]);
#pragma unroll
        for (int i = 0; i < 2; ++i) t[i] += dpp<0x141>(t[i]);
        const float y = dpp<0x128>(hp ? t[0] : t[1]), own = hp ? t[1] : t[0];
        const float x = rbf(hp ? y : own), up = rbf(hp ? own : y);
        const float z = 0.7978845608028654f * (x + 0.044715f * x * x * x);
        const float th = 1.f - 2.f * __builtin_amdgcn_rcpf(__expf(2.f * z) + 1.f);
        const float hv = rbf(0.5f * x * (1.f + th)) * up;
        const unsigned hd = pk_bf(hv, hv), ho = __builtin_amdgcn_mov_dpp(hd, 0x128, 0xf, 0xf, true);
        hd0 = hp ? ho : hd; hd1 = hp ? hd : ho;
        f4 e[5];
        const v4u b0 = diag(hd0, mk), b1 = diag(hd1, mk);
#pragma unroll
        for (int bl = 0; bl < 4; ++bl) e[bl] = mfma_first2(wb[(18 + 2 * bl) % GW], b0, wb[(19 + 2 * bl) % GW], b1);
        e[4] = mfma_first(wb[26 % GW], diag(hd, mk));
        mfma_fence5(e);
        if (!hp) {
#pragma unroll
          for (int bl = 0; bl < 4; ++bl) {
            float* q = (float*)(smem + rc_) + 128 * bl;
#pragma unroll
            for (int r = 0; r < 4; ++r) q[8 * r] = e[bl][r];
          }
        }
        if ((g >> 1) == (c >> 3)) {
          float* q = (float*)(smem + rd);
#pragma unroll
          for (int r = 0; r < 4; ++r) q[8 * r] = e[4][r];
        }
      }
      wg_barrier();
      {
        const f2* rp = (const f2*)(smem + L_REDB);
        const unsigned tg = TAG(ph + 3);
        f2 m[4], x[8];
#pragma unroll
        for (int i = 0; i < 4; ++i) m[i] = rp[256 * i + tid];
#pragma unroll
        for (int i = 0; i < 8; ++i) x[i] = rp[1024 + 32 * i + 8 * w + (lane & 7)];
        const f2 a = (m[0] + m[1]) + (m[2] + m[3]);
        v4u g0 = {__float_as_uint(a[0]), tg, __float_as_uint(a[1]), tg};
        __builtin_amdgcn_raw_buffer_store_b128(g0, rsW, o1, 0, AUX_SC1);
        const f2 e = ((x[0] + x[1]) + (x[2] + x[3])) + ((x[4] + x[5]) + (x[6] + x[7]));
        v4u g1 = {__float_as_uint(e[0]), tg, __float_as_uint(e[1]), tg};
        __builtin_amdgcn_raw_buffer_store_b128(g1, rsW, o2, 0, AUX_SC1);
      }
      if constexpr (layer == 0) issue_wq();
    } else {
      if (myr >= 0) {
        reduce_cols(std::integral_constant<int, NWG>{}, TAG(ph + 1), std::false_type{}, TAG(ph + 2));
      }
      if constexpr (layer == 0) issue_wq();
      if (myr >= 0) {
        reduce_cols(std::integral_constant<int, NMLP>{}, TAG(ph + 3), std::integral_constant<bool, layer == 1>{}, TAG(ph + 4));
        if (layer == 1 && myr == 0 && tid == 0) __builtin_amdgcn_raw_buffer_store_b32(tagbase + 16u, rsW, WS_EPOCH, 0, AUX_SC1);
      }
    }
    if constexpr (layer == 0) {
      relane();
      pin(mk);
      if (w < 3) gather_x_ln(TAG(ph + 4), 48);
      if (w == 0) put_tb(TAG(ph + 5), now8());
      wait_wq<0>(wqf);
      q_gemv();
      wg_barrier();
      if (w == 3) {
        q_publish(TAG(ph + 5));
        __builtin_amdgcn_s_sleep(6);
        v4u a[3];
        poll3(a, rsC, WS_QG + head * 1024 + lane * 16, WS_TB + (lane & 3) * 16, OOB, TAG(ph + 5), 0ull);
        q_keep(a[0]);
        keep_tb(a[1]);
      }
      wg_barrier();
    }
  };

  load_q();
  sfor<0, D>([&](auto I) { def_a(VL[I.value]); });
  float* alp_ks = (float*)(smem + L_KS + w * (NL * 4096) + D * 4096) + lane;
#pragma unroll 1
  for (int i = 0; i < NB1; ++i) block(std::integral_constant<int, NT1>{}, std::true_type{}, alp_ks, NR + i * NT1, PACE_B1, true, [](auto) {});
  sfor<D, 2 * NL>([&](auto Qi) {
    constexpr int q = Qi.value;
    wait_vm<4 * (D - 1)>();
    if constexpr (q - D >= NL) tile_l(st, std::integral_constant<int, q - D - NL>{});
    gate(PACE_B1);
    if constexpr (q < NL) send_k(q, NTILE - NL + q);
    else issue_v<(q - NL < D) ? 2 : 0>(VL[q - NL], voffV, rsV, toff(NTILE - 2 * NL + q));
    if constexpr (q - D < NR) tile_r(st, std::integral_constant<int, q - D>{});
  });
#pragma unroll
  for (int i = 0; i < WARM1; ++i) warm(i);
  sfor<NL - D, NL>([&](auto I) {
    wait_vm<4 * (NL - 1 - I.value) + 3 * WARM1 + (I.value == NL - 1 ? 6 : 0)>();
    tile_l(st, I);
    if constexpr (I.value == NL - 2) load_wo(wo1);
  });
  chain(I0{}, wo1);
  relane();
  due = lds_get(lbase + L_FLAG + 28) + PACE_OFF2 * 256;
  load_q();
  sfor<0, D>([&](auto I) {
    tile_l(st2, I);
    if constexpr (I.value > 0) gate(PACE_A2);
    send_k(I.value, NR + I.value);
  });
  block(std::integral_constant<int, NTA>{}, std::false_type{}, (float*)(smem + L_REDB) + w * (NTA * 64) + lane, NR, PACE_A2, true, [&](auto S) {
    constexpr int nk = NR + NL - D, ink = nk - NTA, s = S.value;
    constexpr int k = s >= NTA ? ink + (s - NTA) : ((s * ink) / NTA != ((s + 1) * ink) / NTA ? (s * ink) / NTA : -1);
    if constexpr (k >= NR) tile_l(st2, std::integral_constant<int, k - NR + D>{});
    else if constexpr (k >= 0) tile_r(st2, std::integral_constant<int, k>{});
  });
  merge_st2();
  due += SLACK2 * 256;
  alp_ks = (float*)(smem + L_KS + w * (NL * 4096) + D * 4096) + lane;
#pragma unroll 1
  for (int i = 0; i < NB2; ++i) block(std::integral_constant<int, NT2>{}, std::false_type{}, alp_ks, NR + NTA + i * NT2, PACE_B2, i < NB2 - 1, [](auto) {});
  load_wo(wo2);
  chain(I1{}, wo2);
  asm volatile(".Lfd_code_to:");
}

}

extern "C" void kernel_launch(void* const* d_in, const int* in_sizes, int n_in, void* d_out, int out_size, void* d_ws, size_t ws_size,
                              hipStream_t stream) {
  (void)in_sizes; (void)n_in; (void)out_size; (void)ws_size;
  fused_decode_kernel<<<NWG, 256, 0, stream>>>(
      (const unsigned short*)d_in[0], (const unsigned char*)d_in[1], (const unsigned char*)d_in[2], (const unsigned char*)d_in[3],
      (const unsigned char*)d_in[4], (const unsigned char*)d_in[6], (const unsigned char*)d_in[7], (const unsigned char*)d_in[8],
      (unsigned short*)d_out, (unsigned char*)d_ws);
}
```

```cpp
#include <hip/hip_runtime.h>
#include <hip/hip_bf16.h>
#include <type_traits>

namespace {

typedef unsigned v4u __attribute__((ext_vector_type(4)));
typedef unsigned v2u __attribute__((ext_vector_type(2)));
typedef int v4i __attribute__((ext_vector_type(4)));
typedef float f4 __attribute__((ext_vector_type(4)));
typedef float f2 __attribute__((ext_vector_type(2)));
typedef __bf16 bf2 __attribute__((ext_vector_type(2)));
#define FI __device__ __forceinline__

constexpr int DM = 576, DFF = 4608, NQ = 4096;
constexpr int NWG = 256;
constexpr int NMLP = 144;
constexpr int NRED = 36;
constexpr int XREP = 8;
constexpr int NTILE = 64;
constexpr int NR = 9;
constexpr int NL = 9;
constexpr int D = 3;
constexpr int NSTREAM = NTILE - NR - NL, NTA = 10, NB1 = 2, NT1 = NSTREAM / NB1, NB2 = 2, NT2 = (NSTREAM - NTA) / NB2;
static_assert(NB1 * NT1 == NSTREAM && NTA + NB2 * NT2 == NSTREAM && D >= 2 && D <= 4 && NR + NL - D <= 2 * NTA && NT1 <= 32 && NT2 <= 32, "schedule");
static_assert(NT1 * 256 <= (NL - D) * 4096 && 2 * NT1 <= (NL - D) * 16, "room for the rescale factors and the parked probabilities");
static_assert(4 * XREP <= 64, "wave 0 writes every copy of x");
constexpr int GATE_WIN = 12;
constexpr int WARM1 = 2;
constexpr int QG_AT = 5;
constexpr unsigned PACE_R = 14208, PACE_B1 = 13632, PACE_A2 = 13824, PACE_B2 = 13632;
constexpr unsigned HEAD_STAGGER = 864;
constexpr int PACE_OFF1 = 0, TB_DELTA = 15, PACE_OFF2 = 135;
static_assert(GATE_WIN >= 9, "all down fragments must fit the window");
constexpr float QSCALE = 0.08838834764831845f * 1.4426950408889634f;
constexpr unsigned SPIN_CAP = 1u << 21;

constexpr unsigned WS_EPOCH = 0;
constexpr unsigned WS_TB = 256;
constexpr unsigned WS_QG = 4096;
constexpr unsigned WS_XG = 32768, XG_STRIDE = 2560;
constexpr unsigned WS_PA = 65536 + 32768, PA_MSG = 160, PA_STRIDE = 16 * PA_MSG;
constexpr unsigned WS_PB = 1u << 20, PB_STRIDE = NWG * 128;
constexpr unsigned WS_BYTES = WS_PB + NRED * PB_STRIDE;
constexpr unsigned OOB = 0x7fffff00u;

constexpr int L_KS = 0;
constexpr int L_SC = 4 * NL * 4096;
constexpr int L_REDB = L_SC;
constexpr int L_REDA = L_SC + 10240;
constexpr int L_XB = L_REDA + 2048;
constexpr int L_QB = L_XB + 1280;
constexpr int L_PAD = L_QB + 512;
constexpr int L_FLAG = L_PAD + 1024;
constexpr int L_XR = L_FLAG + 32;
constexpr int L_BYTES = L_XR + 32;
static_assert(L_BYTES <= 163840, "LDS");

template <int B, int E, class F> FI void sfor(F&& f) {
  if constexpr (B < E) { f(std::integral_constant<int, B>{}); sfor<B + 1, E>(f); }
}

FI v4i mk_rsrc(const void* p, unsigned bytes) {
  unsigned long long a = (unsigned long long)p;
  v4i r = {(int)(unsigned)a, (int)((unsigned)(a >> 32) & 0xffffu), (int)bytes, 0x00020000};
  return r;
}
FI float bf_lo(unsigned u) { return __uint_as_float(u << 16); }
FI float bf_hi(unsigned u) { return __uint_as_float(u & 0xffff0000u); }
FI unsigned pk_bf(float a, float b) { f2 v = {a, b}; return __builtin_bit_cast(unsigned, __builtin_convertvector(v, bf2)); }
FI float rbf(float a) { return bf_lo(pk_bf(a, a)); }
FI float frcp(float x) { const float r = __builtin_amdgcn_rcpf(x); return __builtin_fmaf(__builtin_fmaf(-x, r, 1.f), r, r); }
FI float ex2(float x) { return __builtin_amdgcn_exp2f(x); }

template <int CTRL> FI float dpp(float x) {
  return __builtin_bit_cast(float, __builtin_amdgcn_mov_dpp(__builtin_bit_cast(int, x), CTRL, 0xf, 0xf, true));
}
FI float row_sum(float x) {
  x += dpp<0xB1>(x); x += dpp<0x4E>(x); x += dpp<0x141>(x); x += dpp<0x128>(x); return x;
}
FI float row_max(float x) {
  x = fmaxf(x, dpp<0xB1>(x)); x = fmaxf(x, dpp<0x4E>(x)); x = fmaxf(x, dpp<0x141>(x)); x = fmaxf(x, dpp<0x128>(x)); return x;
}
FI float xrow_max(float x) {
  auto s = __builtin_amdgcn_permlane16_swap(__float_as_uint(x), __float_as_uint(x), false, false);
  x = fmaxf(__uint_as_float(s[0]), __uint_as_float(s[1]));
  auto t = __builtin_amdgcn_permlane32_swap(__float_as_uint(x), __float_as_uint(x), false, false);
  return fmaxf(__uint_as_float(t[0]), __uint_as_float(t[1]));
}
FI float xrow_sum(float x) {
  auto s = __builtin_amdgcn_permlane16_swap(__float_as_uint(x), __float_as_uint(x), false, false);
  x = __uint_as_float(s[0]) + __uint_as_float(s[1]);
  auto t = __builtin_amdgcn_permlane32_swap(__float_as_uint(x), __float_as_uint(x), false, false);
  return __uint_as_float(t[0]) + __uint_as_float(t[1]);
}
FI float wave_sum(float x) { return xrow_sum(row_sum(x)); }
template <int N> FI void row_sums(float (&x)[N]) {
#pragma unroll
  for (int i = 0; i < N; ++i) x[i] += dpp<0xB1>(x[i]);
  __builtin_amdgcn_sched_barrier(0);
#pragma unroll
  for (int i = 0; i < N; ++i) x[i] += dpp<0x4E>(x[i]);
  __builtin_amdgcn_sched_barrier(0);
#pragma unroll
  for (int i = 0; i < N; ++i) x[i] += dpp<0x141>(x[i]);
  __builtin_amdgcn_sched_barrier(0);
#pragma unroll
  for (int i = 0; i < N; ++i) x[i] += dpp<0x128>(x[i]);
  __builtin_amdgcn_sched_barrier(0);
}
FI void xrow_sums2(float& a, float& b) {
  auto s = __builtin_amdgcn_permlane16_swap(__float_as_uint(a), __float_as_uint(b), false, false);
  const float t = __uint_as_float(s[0]) + __uint_as_float(s[1]);
  auto u = __builtin_amdgcn_permlane32_swap(__float_as_uint(t), __float_as_uint(t), false, false);
  const float v = __uint_as_float(u[0]) + __uint_as_float(u[1]);
  auto r = __builtin_amdgcn_permlane16_swap(__float_as_uint(v), __float_as_uint(v), false, false);
  a = __uint_as_float(r[0]); b = __uint_as_float(r[1]);
}
FI void pin(unsigned& a) { asm volatile("" : "+v"(a)); }

FI void wg_barrier() { asm volatile("s_waitcnt lgkmcnt(0)\n\ts_barrier" ::: "memory"); }
template <int N> FI void wait_vm() { asm volatile("s_waitcnt vmcnt(%0)" ::"i"(N > 63 ? 63 : N) : "memory"); }

FI void mfma_acc(f4& d, const v4u& a, const v4u& b) {
  asm("s_nop 1\n\tv_mfma_f32_16x16x32_bf16 %0, %1, %2, %0" : "+v"(d) : "v"(a), "v"(b));
}
FI f4 mfma_first(const v4u& a, const v4u& b) {
  f4 d;
  asm("s_nop 1\n\tv_mfma_f32_16x16x32_bf16 %0, %1, %2, 0" : "=&v"(d) : "v"(a), "v"(b));
  return d;
}
FI void mfma_acc2(f4& d, const v4u& a0, const v4u& b0, const v4u& a1, const v4u& b1) {
  asm("s_nop 1\n\tv_mfma_f32_16x16x32_bf16 %0, %1, %2, %0\n\tv_mfma_f32_16x16x32_bf16 %0, %3, %4, %0" : "+v"(d) : "v"(a0), "v"(b0), "v"(a1), "v"(b1));
}
FI f4 mfma_first2(const v4u& a0, const v4u& b0, const v4u& a1, const v4u& b1) {
  f4 d;
  asm("s_nop 1\n\tv_mfma_f32_16x16x32_bf16 %0, %1, %2, 0\n\tv_mfma_f32_16x16x32_bf16 %0, %3, %4, %0" : "=&v"(d) : "v"(a0), "v"(b0), "v"(a1), "v"(b1));
  return d;
}
FI void mfma_fence(f4& d) { asm("s_nop 7\n\ts_nop 1" : "+v"(d)); }

FI void mfma_fence2(f4& d, f4& e) { asm("s_nop 7\n\ts_nop 1" : "+v"(d), "+v"(e)); }
FI void mfma_fence5(f4 (&e)[5]) { asm("s_nop 7\n\ts_nop 1" : "+v"(e[0]), "+v"(e[1]), "+v"(e[2]), "+v"(e[3]), "+v"(e[4])); }

struct Masks { unsigned m0, m1, m2, m3; };
FI v4u diag(unsigned dup, const Masks& k) { v4u b = {dup & k.m0, dup & k.m1, dup & k.m2, dup & k.m3}; return b; }
FI void pin(Masks& k) { asm volatile("" : "+v"(k.m0), "+v"(k.m1), "+v"(k.m2), "+v"(k.m3)); }
FI unsigned dup_lo(unsigned x) { return __builtin_amdgcn_perm(x, x, 0x01000100u); }
FI unsigned dup_hi(unsigned x) { return __builtin_amdgcn_perm(x, x, 0x03020302u); }

struct Tile { v4u r[4]; };
struct WoFrag { v4u f[4], c[2]; };

struct Ln {
  int lane, g, c;
  Masks mk;
};
FI Ln make_ln() {
  Ln L;
  asm volatile("v_mbcnt_lo_u32_b32 %0, -1, 0\n\tv_mbcnt_hi_u32_b32 %0, -1, %0" : "=v"(L.lane));
  L.g = L.lane >> 4; L.c = L.lane & 15;
  const unsigned hm = (L.c & 1) ? 0xffff0000u : 0x0000ffffu;
  const int e2 = (L.c & 7) >> 1;
  L.mk.m0 = e2 == 0 ? hm : 0u; L.mk.m1 = e2 == 1 ? hm : 0u; L.mk.m2 = e2 == 2 ? hm : 0u; L.mk.m3 = e2 == 3 ? hm : 0u;
  return L;
}

FI void issue_k(unsigned m0v, unsigned voff, const v4i& rs, unsigned soff) {
  unsigned keep, tmp;
  asm volatile(
      "s_nop 1\n\t"
      "s_mov_b32 %0, m0\n\t"
      "s_mov_b32 m0, %2\n\t"
      "v_xor_b32 %1, 64, %3\n\t"
      "buffer_load_dwordx4 %3, %4, %5 offen nt lds\n\t"
      "buffer_load_dwordx4 %1, %4, %5 offen offset:1024 nt lds\n\t"
      "v_xor_b32 %1, 0x80, %3\n\t"
      "buffer_load_dwordx4 %1, %4, %5 offen offset:2048 nt lds\n\t"
      "v_xor_b32 %1, 0xc0, %3\n\t"
      "buffer_load_dwordx4 %1, %4, %5 offen offset:3072 nt lds\n\t"
      "s_mov_b32 m0, %0"
      : "=&s"(keep), "=&v"(tmp)
      : "s"(m0v), "v"(voff), "s"(rs), "s"(soff)
      : "memory");
}
#define V_LOADS \
  "s_nop 4\n\t" \
  "buffer_load_dwordx4 %0, %4, %5, %6 offen nt\n\t" \
  "buffer_load_dwordx4 %1, %4, %5, %6 offen offset:1024 nt\n\t" \
  "buffer_load_dwordx4 %2, %4, %5, %6 offen offset:2048 nt\n\t" \
  "buffer_load_dwordx4 %3, %4, %5, %6 offen offset:3072 nt"
template <int MODE> FI void issue_v(Tile& t, unsigned voff, const v4i& rs, unsigned soff) {
  if constexpr (MODE == 0)
    asm volatile(V_LOADS : "=a"(t.r[0]), "=a"(t.r[1]), "=a"(t.r[2]), "=a"(t.r[3]) : "v"(voff), "s"(rs), "s"(soff) : "memory");
  else if constexpr (MODE == 1)
    asm volatile(V_LOADS : "=&v"(t.r[0]), "=&v"(t.r[1]), "=&v"(t.r[2]), "=&v"(t.r[3]) : "v"(voff), "s"(rs), "s"(soff) : "memory");
  else
    asm volatile(V_LOADS ::"a"(t.r[0]), "a"(t.r[1]), "a"(t.r[2]), "a"(t.r[3]), "v"(voff), "s"(rs), "s"(soff) : "memory");
}
FI void def_a(Tile& t) { asm volatile("" : "=a"(t.r[0]), "=a"(t.r[1]), "=a"(t.r[2]), "=a"(t.r[3])); }
template <bool A> FI unsigned park(unsigned v) {
  if constexpr (A) { unsigned r; asm("v_accvgpr_write_b32 %0, %1" : "=a"(r) : "v"(v)); return r; } else return v;
}
template <bool A> FI unsigned unpark(unsigned r) {
  if constexpr (A) { unsigned v; asm("v_accvgpr_read_b32 %0, %1" : "=v"(v) : "a"(r)); return v; } else return r;
}
FI void touch(unsigned m0v, unsigned voff, const v4i& rs, unsigned soff) {
  unsigned keep;
  asm volatile(
      "s_mov_b32 %0, m0\n\t"
      "s_mov_b32 m0, %1\n\t"
      "s_nop 2\n\t"
      "buffer_load_dword %2, %3, %4 offen lds\n\t"
      "s_mov_b32 m0, %0"
      : "=&s"(keep)
      : "s"(m0v), "v"(voff), "s"(rs), "s"(soff)
      : "memory");
}
FI void touch16(unsigned m0v, unsigned voff, const v4i& rs, unsigned soff) {
  unsigned keep;
  asm volatile(
      "s_mov_b32 %0, m0\n\t"
      "s_mov_b32 m0, %1\n\t"
      "s_nop 2\n\t"
      "buffer_load_dwordx4 %2, %3, %4 offen lds\n\t"
      "s_mov_b32 m0, %0"
      : "=&s"(keep)
      : "s"(m0v), "v"(voff), "s"(rs), "s"(soff)
      : "memory");
}
FI void ride_load(const v4u& q, const v4i& rs, unsigned voff) {
  asm volatile("s_nop 4\n\tbuffer_load_dwordx4 %0, %1, %2, 0 offen sc1" ::"v"(q), "v"(voff), "s"(rs) : "memory");
}
FI v4u ride_take(const v4u& q) {
  v4u r;
  asm volatile("v_mov_b64 %0, %2\n\tv_mov_b64 %1, %3" : "=&v"(*(v2u*)&r), "=&v"(*((v2u*)&r + 1)) : "v"(*(const v2u*)&q), "v"(*((const v2u*)&q + 1)));
  return r;
}
FI void def_v(v4u& t) { asm volatile("" : "=v"(t)); }
FI void ldm3(v4u (&a)[3], const v4i& rs, unsigned v0, unsigned v1, unsigned v2, unsigned long long m) {
  unsigned long long keep;
  asm volatile(
      "s_mov_b64 %3, exec\n\t"
      "s_mov_b64 exec, %8\n\t"
      "s_nop 2\n\t"
      "buffer_load_dwordx4 %0, %5, %4, 0 offen sc1\n\t"
      "buffer_load_dwordx4 %1, %6, %4, 0 offen sc1\n\t"
      "buffer_load_dwordx4 %2, %7, %4, 0 offen sc1\n\t"
      "s_mov_b64 exec, %3\n\t"
      "s_waitcnt vmcnt(0)"
      : "+v"(a[0]), "+v"(a[1]), "+v"(a[2]), "=&s"(keep) : "s"(rs), "v"(v0), "v"(v1), "v"(v2), "s"(m) : "memory");
}
FI unsigned long long untagged(const v4u& a, unsigned tg) {
  return __builtin_amdgcn_ballot_w64(a[1] != tg) | __builtin_amdgcn_ballot_w64(a[3] != tg);
}
FI void poll3(v4u (&a)[3], const v4i& rs, unsigned v0, unsigned v1, unsigned v2, unsigned tg, unsigned long long care2) {
#pragma unroll
  for (int k = 0; k < 3; ++k) a[k] = (v4u){0u, 0u, 0u, 0u};
  unsigned long long miss = ~0ull;
#pragma unroll 1
  for (unsigned spins = 0; spins < SPIN_CAP; ++spins) {
    ldm3(a, rs, v0, v1, v2, miss);
    miss &= untagged(a[0], tg) | untagged(a[1], tg) | (untagged(a[2], tg) & care2);
    if (miss == 0ull) break;
  }
}
#define LDM(i, m) "s_mov_b64 exec, %" #m "\n\tbuffer_load_dwordx4 %" #i ", %7, %6, %5 offen sc1\n\ts_add_u32 %5, %5, 0x1000\n\t"
FI void ldm4(v4u& a0, v4u& a1, v4u& a2, v4u& a3, const v4i& rs, unsigned v0, unsigned so, unsigned long long m0, unsigned long long m1,
             unsigned long long m2, unsigned long long m3) {
  unsigned long long keep;
  asm volatile("s_mov_b64 %4, exec\n\ts_nop 3\n\t" LDM(0, 8) LDM(1, 9) LDM(2, 10) LDM(3, 11) "s_mov_b64 exec, %4"
               : "+v"(a0), "+v"(a1), "+v"(a2), "+v"(a3), "=&s"(keep), "+s"(so)
               : "s"(rs), "v"(v0), "s"(m0), "s"(m1), "s"(m2), "s"(m3)
               : "memory", "scc");
}
FI void pin4(v4u& a0, v4u& a1, v4u& a2, v4u& a3) { asm volatile("s_waitcnt vmcnt(0)" : "+v"(a0), "+v"(a1), "+v"(a2), "+v"(a3) :: "memory"); }
FI void lds_put(unsigned addr, unsigned v) { asm volatile("s_waitcnt lgkmcnt(0)\n\tds_write_b32 %0, %1" ::"v"(addr), "v"(v) : "memory"); }
FI unsigned lds_get(unsigned addr) {
  unsigned v;
  asm volatile("ds_read_b32 %0, %1\n\ts_waitcnt lgkmcnt(0)" : "=v"(v) : "v"(addr) : "memory");
  return __builtin_amdgcn_readfirstlane(v);
}
template <int SLOT> FI void read_k(Tile& k, unsigned addr) {
  unsigned tmp;
  asm volatile(
      "ds_read_b128 %0, %5 offset:%6\n\t"
      "v_xor_b32 %4, 16, %5\n\t"
      "ds_read_b128 %1, %4 offset:%6\n\t"
      "v_xor_b32 %4, 32, %5\n\t"
      "ds_read_b128 %2, %4 offset:%6\n\t"
      "v_xor_b32 %4, 48, %5\n\t"
      "ds_read_b128 %3, %4 offset:%6\n\t"
      "s_waitcnt lgkmcnt(0)"
      : "=&v"(k.r[0]), "=&v"(k.r[1]), "=&v"(k.r[2]), "=&v"(k.r[3]), "=&v"(tmp)
      : "v"(addr), "i"(SLOT * 4096)
      : "memory");
}
FI f4 qk(const Tile& k, const Tile& q) {
  f4 s;
  asm volatile(
      "v_mfma_f32_16x16x32_bf16 %0, %1, %5, 0\n\t"
      "v_mfma_f32_16x16x32_bf16 %0, %2, %6, %0\n\t"
      "v_mfma_f32_16x16x32_bf16 %0, %3, %7, %0\n\t"
      "v_mfma_f32_16x16x32_bf16 %0, %4, %8, %0\n\t"
      "s_nop 7\n\ts_nop 1"
      : "=&v"(s)
      : "v"(k.r[0]), "v"(k.r[1]), "v"(k.r[2]), "v"(k.r[3]), "v"(q.r[0]), "v"(q.r[1]), "v"(q.r[2]), "v"(q.r[3]));
  return s;
}
FI void load_b64(v2u& d, unsigned voff, const v4i& rs, unsigned soff) {
  asm volatile("s_nop 4\n\tbuffer_load_dwordx2 %0, %1, %2, %3 offen nt" : "=v"(d) : "v"(voff), "s"(rs), "s"(soff) : "memory");
}
template <int N> FI void wait_wq(v2u (&q)[12]) {
  asm volatile("s_waitcnt vmcnt(%12)"
               : "+v"(q[0]), "+v"(q[1]), "+v"(q[2]), "+v"(q[3]), "+v"(q[4]), "+v"(q[5]), "+v"(q[6]), "+v"(q[7]), "+v"(q[8]), "+v"(q[9]),
                 "+v"(q[10]), "+v"(q[11])
               : "i"(N) : "memory");
}

struct Soft { f4 acc; float mc, l; };

template <bool LATER> FI v2u softmax_tile(Soft& st, const f4& s, float* slot, unsigned& moved, unsigned bit) {
  f4 t;
#pragma unroll
  for (int r = 0; r < 4; ++r) t[r] = __builtin_fmaf(s[r], QSCALE, -st.mc);
  const float tm = fmaxf(fmaxf(t[0], t[1]), fmaxf(t[2], t[3]));
  if (__builtin_amdgcn_ballot_w64(tm > 0.f) != 0ull) {
    const float sm = fmaxf(fmaxf(s[0], s[1]), fmaxf(s[2], s[3])) * QSCALE;
    const float mn = fmaxf(st.mc, xrow_max(sm));
    const float al = ex2(st.mc - mn);
    st.mc = mn;
    st.l *= al;
    if constexpr (LATER) { *slot = al; moved |= bit; } else st.acc *= al;
#pragma unroll
    for (int r = 0; r < 4; ++r) t[r] = __builtin_fmaf(s[r], QSCALE, -mn);
  }
  f4 p;
#pragma unroll
  for (int r = 0; r < 4; ++r) p[r] = ex2(t[r]);
  st.l += (p[0] + p[1]) + (p[2] + p[3]);
  v2u pp = {pk_bf(p[0], p[1]), pk_bf(p[2], p[3])};
  return pp;
}
#define PV_MFMAS \
  "s_nop 1\n\t" \
  "v_mfma_f32_16x16x32_bf16 %0, %1, %5, %0\n\t" \
  "v_mfma_f32_16x16x32_bf16 %0, %2, %6, %0\n\t" \
  "v_mfma_f32_16x16x32_bf16 %0, %3, %7, %0\n\t" \
  "v_mfma_f32_16x16x32_bf16 %0, %4, %8, %0\n\t" \
  "s_nop 7\n\ts_nop 1"
template <bool A> FI void pv_tile(Soft& st, const v2u& p, const Tile& v, const Masks& k) {
  const v4u b0 = diag(dup_lo(p[0]), k), b1 = diag(dup_hi(p[0]), k), b2 = diag(dup_lo(p[1]), k), b3 = diag(dup_hi(p[1]), k);
  if constexpr (A)
    asm volatile(PV_MFMAS : "+v"(st.acc) : "a"(v.r[0]), "a"(v.r[1]), "a"(v.r[2]), "a"(v.r[3]), "v"(b0), "v"(b1), "v"(b2), "v"(b3));
  else
    asm volatile(PV_MFMAS : "+v"(st.acc) : "v"(v.r[0]), "v"(v.r[1]), "v"(v.r[2]), "v"(v.r[3]), "v"(b0), "v"(b1), "v"(b2), "v"(b3));
}

constexpr bool vr_in_a(int i) { return i < 7; }

constexpr int imin(int a, int b) { return a < b ? a : b; }
constexpr int imax(int a, int b) { return a > b ? a : b; }

constexpr int AUX_SC1 = 16, AUX_NT = 2;

__global__ void __launch_bounds__(256, 1)
fused_decode_kernel(const unsigned short* __restrict__ xin, const unsigned char* __restrict__ kc, const unsigned char* __restrict__ vc,
                    const unsigned char* __restrict__ wq, const unsigned char* __restrict__ wo, const unsigned char* __restrict__ wg,
                    const unsigned char* __restrict__ wu, const unsigned char* __restrict__ wd, unsigned short* __restrict__ out,
                    unsigned char* ws) {
  __shared__ __attribute__((aligned(16))) unsigned char smem[L_BYTES];
  const int b = blockIdx.x;
  const int w = __builtin_amdgcn_readfirstlane(threadIdx.x >> 6);
  const int head = b >> 4, split = b & 15;
  const int myr = b - (NWG - NRED);
  const unsigned lbase = (unsigned)(size_t)smem;

  const v4i rsK = mk_rsrc(kc + ((size_t)head << 24), 1u << 24);
  const v4i rsV = mk_rsrc(vc + ((size_t)head << 24), 1u << 24);
  const v4i rsQ = mk_rsrc(wq, DM * NQ * 2);
  const v4i rsC = mk_rsrc(ws, WS_BYTES);
  const __amdgpu_buffer_rsrc_t rsW = __builtin_amdgcn_make_buffer_rsrc(ws, 0, WS_BYTES, 0x00020000);

  const unsigned m0base = lbase + L_KS + w * (NL * 4096);
  const unsigned soffw = split * 16384 + w * 4096;
  auto toff = [&](unsigned t) { return soffw + (((t + head * 16u) & 63u) << 18); };
  int lane, g, c, tid;
  Masks mk;
  unsigned voffV, voffK, addrK;
  auto relane = [&]() {
    const Ln L = make_ln();
    lane = L.lane; g = L.g; c = L.c; mk = L.mk; tid = w * 64 + lane;
    voffV = lane * 16;
    voffK = g * 256 + ((c ^ g) << 4);
    const unsigned kap = 4 * (c & 3) + (c >> 2);
    addrK = m0base + kap * 256 + (((4 * g) ^ kap) << 4);
  };
  relane();

  Tile KR[NR], VR[NR], VL[NL];

  auto now8 = [&]() { return (unsigned)__builtin_amdgcn_s_memrealtime() << 8; };
  unsigned t_go = 0, due = 0;
  auto gate = [&](unsigned pace) {
    due += pace;
    while ((int)(now8() - due) < 0) __builtin_amdgcn_s_sleep(1);
  };
  auto send_k = [&](int slot, unsigned t) { issue_k(m0base + slot * 4096, voffK, rsK, toff(t)); };

  const int qs = (b & 7) * 32 + (b >> 3);
  v2u wqf[12];
  auto issue_wq = [&]() {
    const unsigned voffQ = (192u * w + (4 * g + (c >> 2)) * 12u) * (NQ * 2) + (unsigned)(qs * 16 + 4 * (c & 3)) * 2u;
    sfor<0, 12>([&](auto I) {
      load_b64(wqf[I.value], voffQ, rsQ, I.value * NQ * 2);
    });
  };

  auto xpiece = [&](int np, int k) { const unsigned j = np * w + lane + 64 * k; return j >= 144u ? j - 144u : j; };
  auto layernorm = [&](v2u (&px)[3], int np) {
    const bool act2 = lane < 16;
    auto up = [](unsigned pr) { return (f2){bf_lo(pr), bf_hi(pr)}; };
    const float x0 = bf_lo(__builtin_amdgcn_readfirstlane(px[0][0]));
    f2 s = {0.f, 0.f}, q = {0.f, 0.f};
#pragma unroll
    for (int k = 0; k < 3; ++k) {
#pragma unroll
      for (int i = 0; i < 2; ++i) {
        f2 a = up(px[k][i]) - x0;
        if (k == 2) a = act2 ? a : (f2){0.f, 0.f};
        s += a;
        q = __builtin_elementwise_fma(a, a, q);
      }
    }
    float sq[2] = {s[0] + s[1], q[0] + q[1]};
    row_sums(sq);
    xrow_sums2(sq[0], sq[1]);
    const float md = sq[0] * (1.f / DM);
    const float mean = x0 + md;
    const float var = fmaxf(__builtin_fmaf(-md, md, sq[1] * (1.f / DM)), 0.f);
    const float mb = rbf(mean);
    const float den = rbf(__builtin_amdgcn_sqrtf(rbf(rbf(var) + 1e-5f)));
    const float rc = __builtin_amdgcn_rcpf(den);
    auto dv = [&](unsigned pr) {
      const unsigned a2 = pk_bf(bf_lo(pr) - mb, bf_hi(pr) - mb);
      const f2 a = {bf_lo(a2), bf_hi(a2)};
      const f2 q0 = a * rc;
      const f2 q1 = __builtin_elementwise_fma(__builtin_elementwise_fma(-q0, (f2){den, den}, a), (f2){rc, rc}, q0);
      return pk_bf(q1[0], q1[1]);
    };
    v2u o = {dv(px[0][0]), dv(px[0][1])};
    if (lane < np) *(v2u*)(smem + L_XB + (np * w + lane) * 8) = o;
  };

  auto q_gemv = [&]() {
    if (w < 3) {
      const v2u* xp = (const v2u*)(smem + L_XB + (192 * w + (4 * g + (c >> 2)) * 12) * 2);
      const v2u xa = xp[0], xb = xp[1], xc = xp[2];
      const unsigned xs[6] = {xa[0], xa[1], xb[0], xb[1], xc[0], xc[1]};
      const unsigned ma = mk.m0 | mk.m2, mb = mk.m1 | mk.m3;
      f4 d;
#pragma unroll
      for (int n = 0; n < 6; ++n) {
        const unsigned lo = dup_lo(xs[n]), hi = dup_hi(xs[n]);
        const v4u bb = {lo & ma, lo & mb, hi & ma, hi & mb};
        const v4u aa = {wqf[2 * n][0], wqf[2 * n][1], wqf[2 * n + 1][0], wqf[2 * n + 1][1]};
        if (n == 0) d = mfma_first(aa, bb); else mfma_acc(d, aa, bb);
      }
      mfma_fence(d);
      if ((c >> 2) == g) {
        float* rp = (float*)(smem + L_REDA) + (w * 4 + g) * 16 + (c & 3);
#pragma unroll
        for (int r = 0; r < 4; ++r) rp[4 * r] = d[r];
      }
    }
  };
  auto q_publish = [&](unsigned tag) {
    const f2* rp = (const f2*)(smem + L_REDA) + (lane & 7);
    f2 a[12];
#pragma unroll
    for (int i = 0; i < 12; ++i) a[i] = rp[8 * i];
    const f2 t = (((a[0] + a[1]) + (a[2] + a[3])) + ((a[4] + a[5]) + (a[6] + a[7]))) + ((a[8] + a[9]) + (a[10] + a[11]));
    v2u gr = {pk_bf(t[0], t[1]), tag};
    __builtin_amdgcn_raw_buffer_store_b64(gr, rsW, lane < 8 ? WS_QG + (qs * 8 + lane) * 8 : OOB, 0, AUX_SC1);
  };
  auto q_keep = [&](const v4u& v) { v2u o = {v[0], v[2]}; *(v2u*)(smem + L_QB + lane * 8) = o; };

  unsigned long long pc;
  unsigned code_len;
  asm volatile("s_getpc_b64 %0\n.Lfd_code_from:\n\ts_mov_b32 %1, .Lfd_code_to-.Lfd_code_from" : "=s"(pc), "=s"(code_len));
  auto warm_code = [&]() { touch(lbase + L_PAD, lane * 64, mk_rsrc((const void*)pc, code_len), (b >> 3) * 4096); };
  auto put_tb = [&](unsigned tag, unsigned t) {
    if (b >= NWG - 8 && lane == 0) { v2u tb = {t, tag}; __builtin_amdgcn_raw_buffer_store_b64(tb, rsW, WS_TB + (b & 7) * 8, 0, AUX_SC1); }
  };
  auto keep_tb = [&](const v4u& tb) {
    int d = max((int)(tb[0] - t_go), (int)(tb[2] - t_go));
    d = max(d, __builtin_amdgcn_mov_dpp(d, 0xB1, 0xf, 0xf, true));
    d = max(d, __builtin_amdgcn_mov_dpp(d, 0x4E, 0xf, 0xf, true));
    lds_put(lbase + L_FLAG + 28, t_go + d);
  };

  unsigned tagbase;
  auto TAG = [&](unsigned ph) { return 0x80000000u | ((tagbase + ph) & 0x7fffffffu); };
  auto lds_wait = [&](int word) { for (unsigned i = 0; i < 8u * SPIN_CAP && lds_get(lbase + L_FLAG + 4 * word) == 0u; ++i) __builtin_amdgcn_s_sleep(1); };
  auto head_step = [&](auto Qi) {
    constexpr int q = Qi.value;
    if constexpr (q == D) { if (w != 3) { wait_vm<4 * (D - 1) + 12>(); t_go = now8(); } due = t_go + PACE_OFF1 * 256; }
    if constexpr (q >= D) wait_vm<4 * (D - 1) + (q - D < D ? 12 : 0)>();
    gate(PACE_R);
    if constexpr (q < NR) send_k(q, q);
    else if constexpr (q < 2 * NR) { issue_v<vr_in_a(q - NR) ? 0 : 1>(VR[q - NR], voffV, rsV, toff(q - NR)); read_k<q - NR>(KR[q - NR], addrK); }
    else send_k(q - 2 * NR, q - NR);
  };
  {
    v2u px[3];
    unsigned ep;
    send_k(0, 0);
    __builtin_amdgcn_sched_barrier(0);
    {
      const v4u z = {0u, 0u, 0u, 0u};
      if (tid < 144) __builtin_amdgcn_raw_buffer_store_b128(z, rsW, WS_PA + (head * 16 + tid / 9) * PA_STRIDE + split * PA_MSG + (tid % 9) * 16, 0, AUX_SC1);
      __builtin_amdgcn_raw_buffer_store_b128(z, rsW, WS_PB + (tid >> 3) * PB_STRIDE + b * 128 + (tid & 7) * 16, 0, AUX_SC1);
      if (tid < 32) __builtin_amdgcn_raw_buffer_store_b128(z, rsW, WS_PB + (32 + (tid >> 3)) * PB_STRIDE + b * 128 + (tid & 7) * 16, 0, AUX_SC1);
      if (myr >= 0 && tid < 4 * XREP) __builtin_amdgcn_raw_buffer_store_b128(z, rsW, WS_XG + (tid >> 2) * XG_STRIDE + myr * 64 + (tid & 3) * 16, 0, AUX_SC1);
      asm volatile("" ::: "memory");
    }
    const v4i rsX = mk_rsrc(xin, DM * 2);
    const v4i rsE = mk_rsrc(ws, 64);
    asm volatile("buffer_load_dwordx2 %0, %4, %7, 0 offen\n\t"
                 "buffer_load_dwordx2 %1, %5, %7, 0 offen\n\t"
                 "buffer_load_dwordx2 %2, %6, %7, 0 offen\n\t"
                 "buffer_load_dword %3, off, %8, 0 sc1"
                 : "=&v"(px[0]), "=&v"(px[1]), "=&v"(px[2]), "=&v"(ep)
                 : "v"(xpiece(48, 0) * 8), "v"(xpiece(48, 1) * 8), "v"(lane < 16 ? xpiece(48, 2) * 8 : OOB), "s"(rsX), "s"(rsE) : "memory");
    sfor<1, D>([&](auto I) { send_k(I.value, I.value); });
    issue_wq();
    if (w == 2) warm_code();
    if (tid < 8) *(unsigned*)(smem + L_FLAG + 4 * tid) = 0u;
    wg_barrier();
    if (w == 3) {
      asm volatile("s_waitcnt vmcnt(%4)" : "+v"(px[0]), "+v"(px[1]), "+v"(px[2]), "+v"(ep) : "i"(4 * (D - 1) + 12) : "memory");
      tagbase = __builtin_amdgcn_readfirstlane(ep);
      t_go = now8();
      put_tb(TAG(12), t_go);
#pragma unroll
      for (int k = 0; k < 3; ++k) if (((lane + 64 * k) >> 2) == myr) *(v2u*)(smem + L_XR + (lane & 3) * 8) = px[k];
    }
    sfor<D, QG_AT>(head_step);
    wait_wq<4 * (QG_AT - D)>(wqf);
    asm volatile("" : "+v"(ep), "+v"(px[0]), "+v"(px[1]), "+v"(px[2]));
    tagbase = __builtin_amdgcn_readfirstlane(ep);
    if (w < 3) layernorm(px, 48);
  }
  if (w < 3) {
    q_gemv();
    lds_put(lbase + L_FLAG + 4 + 4 * w, 1u);
  }
  v4u ride;
  def_v(ride);
  if (w == 3) ride_load(ride, rsC, WS_TB + (lane & 3) * 16);
  sfor<QG_AT, QG_AT + D + 1>(head_step);
  if (w == 3) {
    v4u a[3];
    a[0] = ride_take(ride);
    if (untagged(a[0], TAG(12)) != 0ull) poll3(a, rsC, WS_TB + (lane & 3) * 16, WS_TB + (lane & 3) * 16, OOB, TAG(12), 0ull);
    keep_tb(a[0]);
    lds_put(lbase + L_FLAG + 20, 1u);
    lds_wait(1); lds_wait(2); lds_wait(3);
    q_publish(TAG(1));
  }
  lds_wait(5);
  due += lds_get(lbase + L_FLAG + 28) + TB_DELTA * 256 - t_go + head * HEAD_STAGGER;
  sfor<QG_AT + D + 1, 2 * NR - 2>(head_step);
  if (w == 3) ride_load(ride, rsC, WS_QG + head * 1024 + lane * 16);
  sfor<2 * NR - 2, 2 * NR + D>(head_step);
  if (w == 3) {
    v4u a[3];
    a[0] = ride_take(ride);
    if (untagged(a[0], TAG(1)) != 0ull) poll3(a, rsC, WS_QG + head * 1024 + lane * 16, WS_QG + head * 1024 + lane * 16, OOB, TAG(1), 0ull);
    q_keep(a[0]);
    lds_put(lbase + L_FLAG + 16, 1u);
  }
  lds_wait(4);

  Tile Q;
  Soft st, st2;
  unsigned moved;
  auto load_q = [&]() {
    const v4u* qp = (const v4u*)(smem + L_QB + (c >> 3) * 256 + g * 64);
#pragma unroll
    for (int j = 0; j < 4; ++j) Q.r[j] = qp[j];
    st.acc = (f4){0.f, 0.f, 0.f, 0.f};
    st.mc = -__builtin_inff();
    st.l = 0.f;
    st2 = st;
  };
  auto merge_st2 = [&]() {
    const float m = fmaxf(st.mc, st2.mc), a1 = ex2(st.mc - m), a2 = ex2(st2.mc - m);
    st.acc = st.acc * a1 + st2.acc * a2;
    st.l = st.l * a1 + st2.l * a2;
    st.mc = m;
  };
  auto tile_r = [&](Soft& s, auto I) { pv_tile<vr_in_a(I.value)>(s, softmax_tile<false>(s, qk(KR[I.value], Q), nullptr, moved, 0u), VR[I.value], mk); };
  auto tile_l = [&](Soft& s, auto I) {
    Tile kt;
    read_k<I.value>(kt, addrK);
    pv_tile<true>(s, softmax_tile<false>(s, qk(kt, Q), nullptr, moved, 0u), VL[I.value], mk);
  };
  WoFrag wo1, wo2;
  auto load_wo = [&](WoFrag& x) {
    const __amdgpu_buffer_rsrc_t r = __builtin_amdgcn_make_buffer_rsrc((void*)wo, 0, DM * NQ * 2, 0x00020000);
    const unsigned p = (b * 16 + 4 * g) * (DM * 2);
#pragma unroll
    for (int n = 0; n < 4; ++n) x.f[n] = __builtin_amdgcn_raw_buffer_load_b128(r, p + n * (DM * 2) + 256 * w + 16 * c, 0, AUX_NT);
#pragma unroll
    for (int n = 0; n < 2; ++n) x.c[n] = __builtin_amdgcn_raw_buffer_load_b128(r, p + (2 * n + (c >> 3)) * (DM * 2) + 1024 + 16 * (c & 7), 0, AUX_NT);
    asm volatile("" ::: "memory");
  };
  auto block = [&](auto NTc, auto PARKc, float* alp, unsigned t0, unsigned pace, bool more, auto&& side) {
    constexpr int NT = NTc.value;
    constexpr bool PARK = PARKc.value;
    v2u pend[NT];
    moved = 0u;
    sfor<0, 2 * NT>([&](auto Sc) {
      constexpr int s = Sc.value, q = s + D;
      if constexpr (s + D - 1 < 2 * NT) wait_vm<4 * (D - 1)>();
      else { if (more) wait_vm<4 * (D - 1)>(); else wait_vm<4 * (2 * NT - 1 - s)>(); }
      if constexpr (s < NT) {
        Tile kt;
        read_k<s % D>(kt, addrK);
        if constexpr (q < NT) { gate(pace); send_k(q % D, t0 + q); }
        else { gate(pace); issue_v<2>(VL[(q - NT) % D], voffV, rsV, toff(t0 + q - NT)); }
        const v2u pp = softmax_tile<true>(st, qk(kt, Q), alp + s * 64, moved, 1u << s);
        pend[s][0] = park<PARK>(pp[0]); pend[s][1] = park<PARK>(pp[1]);
      } else {
        constexpr int j = s - NT;
        if (moved & (1u << j)) st.acc *= alp[j * 64];
        const v2u pp = {unpark<PARK>(pend[j][0]), unpark<PARK>(pend[j][1])};
        pv_tile<true>(st, pp, VL[j % D], mk);
        if constexpr (q < 2 * NT) { gate(pace); issue_v<2>(VL[(q - NT) % D], voffV, rsV, toff(t0 + q - NT)); }
        else if (more) { gate(pace); send_k(q - 2 * NT, t0 + NT + q - 2 * NT); }
      }
      side(Sc);
    });
  };
  using I0 = std::integral_constant<int, 0>;
  using I1 = std::integral_constant<int, 1>;

  auto warm = [&](int wi) {
    const v4i rsG = mk_rsrc(wg, DM * DFF * 2), rsU = mk_rsrc(wu, DM * DFF * 2), rsD = mk_rsrc(wd, DM * DFF * 2);
    const unsigned q = (b >> 3) * 1536u + tid + 256 * wi, r = q / 72u;
    const unsigned vg = r * (DFF * 2) + (q - r * 72u) * 16u;
    touch16(lbase + L_PAD, vg, rsG, (b & 7) * 1152u);
    touch16(lbase + L_PAD, vg, rsU, (b & 7) * 1152u);
    touch16(lbase + L_PAD, q < 41472u ? q * 16u : OOB, rsD, (b & 7) * (576u * DM * 2));
  };
  auto chain = [&](auto LAYER, const WoFrag& wf) {
    constexpr int layer = LAYER.value;
    relane();
    const unsigned ph = 2 + 5 * layer;

    const int mu = (b & 7) * 18 + (b >> 3);
    constexpr int GW = layer == 0 ? GATE_WIN : 27;
    v4u wb[GW];
    const unsigned char* pgu;
    unsigned odn, odc;
    auto bases = [&]() {
      pgu = (((c >> 2) & 1) ? wu : wg) + (size_t)(144 * w + (2 * g + (c >> 3)) * 18) * (DFF * 2) + 64 * mu + 16 * (c & 3);
      odn = (32 * mu + 8 * w + 2 * g) * (DM * 2) + 16 * c;
      odc = (32 * mu + 8 * w + 2 * g + (c >> 3)) * (DM * 2) + 1024 + 16 * (c & 7);
    };
    const __amdgpu_buffer_rsrc_t rsDn = __builtin_amdgcn_make_buffer_rsrc((void*)wd, 0, DM * DFF * 2, 0x00020000);
    auto frag = [&](auto F) {
      constexpr int f = F.value;
      if constexpr (f < 18) wb[f % GW] = *(const v4u*)(pgu + (size_t)f * (DFF * 2));
      else if constexpr (f < 26) wb[f % GW] = __builtin_amdgcn_raw_buffer_load_b128(rsDn, odn + ((f - 18) & 1) * (DM * 2) + ((f - 18) >> 1) * 256, 0, 0);
      else if constexpr (f == 26) wb[f % GW] = __builtin_amdgcn_raw_buffer_load_b128(rsDn, odc, 0, 0);
    };
    {
      const float lt = xrow_sum(st.l);
      float* cp = (float*)(smem + L_REDB) + w * (NTA * 64);
#pragma unroll
      for (int r = 0; r < 4; ++r) cp[(c >> 3) * 128 + 32 * g + 8 * r + (c & 7)] = st.acc[r];
      if ((lane & 0x37) == 0) { f2 ml = {st.mc, lt}; *(f2*)(cp + 256 + 2 * (c >> 3)) = ml; }
    }
    wg_barrier();
    {
      const float* cp = (const float*)(smem + L_REDB);
      const int h = tid >> 7;
      f2 ml[4];
#pragma unroll
      for (int i = 0; i < 4; ++i) ml[i] = *(const f2*)(cp + i * (NTA * 64) + 256 + 2 * h);
      const float M = fmaxf(fmaxf(ml[0][0], ml[1][0]), fmaxf(ml[2][0], ml[3][0]));
      float O = 0.f, L = 0.f;
#pragma unroll
      for (int i = 0; i < 4; ++i) { const float sc = ex2(ml[i][0] - M); O += sc * cp[i * (NTA * 64) + tid]; L += sc * ml[i][1]; }
      const unsigned tg = TAG(ph);
      const unsigned pa = WS_PA + (head * 16 + (tid >> 4)) * PA_STRIDE + split * PA_MSG;
      v2u gr = {__float_as_uint(O), tg};
      __builtin_amdgcn_raw_buffer_store_b64(gr, rsW, pa + c * 8, 0, AUX_SC1);
      if (c == 0) {
        v4u g2 = {__float_as_uint(M), tg, __float_as_uint(L), tg};
        __builtin_amdgcn_raw_buffer_store_b128(g2, rsW, pa + 128, 0, AUX_SC1);
      }
    }
    relane();
    {
      const unsigned tg = TAG(ph);
      const unsigned base = WS_PA + b * PA_STRIDE + c * PA_MSG;
      v4u a[3];
      const unsigned tb = TAG(ph + 1);
      const bool hp = (c >> 3) != 0, odd = (c & 1) != 0;
      const unsigned lo_ = WS_PB + b * 128 + (odd ? 64 : 0) + (c & 6) * 8;
      unsigned ob1 = lo_ + (8 * w + 2 * g + (hp ? 1 : 0)) * PB_STRIDE, ob2 = g == (w >> 1) && !hp ? lo_ + (32 + w) * PB_STRIDE : OOB;
      pin(mk); pin(ob1); pin(ob2);
      __builtin_amdgcn_s_sleep(6);
      poll3(a, rsC, base + 32 * g, base + 32 * g + 16, base + 128, tg, ~0ull);
      const float ms = __uint_as_float(a[2][0]);
      const float wgt = ex2(ms - row_max(ms));
      float o[5] = {wgt * __uint_as_float(a[0][0]), wgt * __uint_as_float(a[0][2]), wgt * __uint_as_float(a[1][0]), wgt * __uint_as_float(a[1][2]),
                    wgt * __uint_as_float(a[2][2])};
      row_sums(o);
      const float inv = frcp(o[4]);
#pragma unroll
      for (int n = 0; n < 4; ++n) o[n] *= inv;
      auto put_line = [&](float lo, float hi, unsigned off) {
        const float lon = dpp<0xB1>(lo), hin = dpp<0xB1>(hi);
        v4u gr = {__float_as_uint(odd ? hin : lo), tb, __float_as_uint(odd ? hi : lon), tb};
        __builtin_amdgcn_raw_buffer_store_b128(gr, rsW, off, 0, AUX_SC1);
      };
      const float oa = hp ? o[1] : o[0], obv = hp ? o[3] : o[2];
      f4 e = mfma_first(wf.c[0], diag(pk_bf(oa, oa), mk));
      mfma_acc(e, wf.c[1], diag(pk_bf(obv, obv), mk));
      f4 d = mfma_first(wf.f[0], diag(pk_bf(o[0], o[0]), mk));
#pragma unroll
      for (int n = 1; n < 4; ++n) mfma_acc(d, wf.f[n], diag(pk_bf(o[n], o[n]), mk));
      mfma_fence2(d, e);
      put_line(hp ? d[2] : d[0], hp ? d[3] : d[1], ob1);
      {
        const float lo = (w & 1) ? e[2] : e[0], hi = (w & 1) ? e[3] : e[1];
        float f[2] = {(g >> 1) == (hp ? 1 : 0) ? lo : 0.f, (g >> 1) == (hp ? 1 : 0) ? hi : 0.f};
#pragma unroll
        for (int r = 0; r < 2; ++r) {
          const float t = f[r] + dpp<0x128>(f[r]);
          auto sw = __builtin_amdgcn_permlane32_swap(__float_as_uint(t), __float_as_uint(t), false, false);
          f[r] = __uint_as_float(sw[0]) + __uint_as_float(sw[1]);
        }
        put_line(f[0], f[1], ob2);
      }
    }
    relane();
    if (layer == 0 && myr < 0) {
#pragma unroll
      for (int i = WARM1; i < 6; ++i) warm(i);
      if (w == 2) warm_code();
    }
    auto reduce_cols = [&](auto NPc, unsigned tg, auto FINAL, unsigned tgx) {
      constexpr int NP = NPc.value, NK = (NP * 8 + 255) / 256;
      v4u a[8];
      unsigned long long miss[8];
#pragma unroll
      for (int k = 0; k < 8; ++k) { a[k] = (v4u){0u, 0u, 0u, 0u}; miss[k] = __builtin_amdgcn_ballot_w64(tid + 256 * k < NP * 8); }
      const unsigned base = WS_PB + myr * PB_STRIDE;
      v2u xo = *(const v2u*)(smem + L_XR + (tid & 3) * 8);
      unsigned ox = FINAL.value ? (tid < 4 ? 32 * myr + 8 * tid : OOB) : (tid < 4 * XREP ? WS_XG + (tid >> 2) * XG_STRIDE + myr * 64 + (tid & 3) * 16 : OOB);
      unsigned x0 = xo[0], x1 = xo[1], ra = L_REDA + (tid & 3) * 16;
      pin(x0); pin(x1); pin(ox); pin(ra);
      if constexpr (NP == NWG) __builtin_amdgcn_s_sleep(12);
#pragma unroll 1
      for (unsigned spins = 0; spins < SPIN_CAP; ++spins) {
        ldm4(a[0], a[1], a[2], a[3], rsC, tid * 16, base, miss[0], miss[1], miss[2], miss[3]);
        if ((miss[4] | miss[5] | miss[6] | miss[7]) != 0ull) ldm4(a[4], a[5], a[6], a[7], rsC, tid * 16, base + 16384, miss[4], miss[5], miss[6], miss[7]);
        pin4(a[4], a[5], a[6], a[7]);
        pin4(a[0], a[1], a[2], a[3]);
        unsigned long long any = 0ull;
#pragma unroll
        for (int k = 0; k < NK; ++k) { miss[k] &= untagged(a[k], tg); any |= miss[k]; }
        if (any == 0ull) break;
      }
      float s0 = __uint_as_float(a[0][0]), s1 = __uint_as_float(a[0][2]);
#pragma unroll
      for (int k = 1; k < NK; ++k) { s0 += __uint_as_float(a[k][0]); s1 += __uint_as_float(a[k][2]); }
      s0 += dpp<0x128>(s0); s1 += dpp<0x128>(s1);
      xrow_sums2(s0, s1);
      if (lane < 8) ((f2*)(smem + L_REDA))[w * 8 + lane] = (f2){s0, s1};
      wg_barrier();
      if (w == 0) {
        const f4* rp = (const f4*)(smem + ra);
        const f4 t = (rp[0] + rp[4]) + (rp[8] + rp[12]);
        const unsigned t0 = pk_bf(t[0], t[1]), t1 = pk_bf(t[2], t[3]);
        const v2u xn = {pk_bf(bf_lo(x0) + bf_lo(t0), bf_hi(x0) + bf_hi(t0)), pk_bf(bf_lo(x1) + bf_lo(t1), bf_hi(x1) + bf_hi(t1))};
        if constexpr (FINAL.value) {
          __builtin_amdgcn_raw_buffer_store_b64(xn, __builtin_amdgcn_make_buffer_rsrc(out, 0, DM * 2, 0x00020000), ox, 0, 0);
        } else {
          const v4u gx = {xn[0], tgx, xn[1], tgx};
          __builtin_amdgcn_raw_buffer_store_b128(gx, rsW, ox, 0, AUX_SC1);
          if (tid < 4) *(v2u*)(smem + L_XR + tid * 8) = xn;
        }
      }
    };
    auto gather_x_ln = [&](unsigned tg, int np) {
      v4u a[3];
      const unsigned xb = WS_XG + (b % XREP) * XG_STRIDE;
      poll3(a, rsC, xb + xpiece(np, 0) * 16, xb + xpiece(np, 1) * 16, lane < 16 ? xb + xpiece(np, 2) * 16 : OOB, tg, 0xffffull);
      v2u px[3];
#pragma unroll
      for (int k = 0; k < 3; ++k) { px[k][0] = a[k][0]; px[k][1] = a[k][2]; }
      layernorm(px, np);
    };

    if (b < NMLP) {
      bases();
      if constexpr (layer == 0) wait_vm<0>();
      sfor<0, GW>(frag);
      unsigned xoff = L_XB + (144 * w + (2 * g + (c >> 3)) * 18) * 2;
      if constexpr (layer != 0) { pin(mk); pin(xoff); }
      gather_x_ln(TAG(ph + 2), 36);
      if constexpr (layer == 0) { relane(); bases(); xoff = L_XB + (144 * w + (2 * g + (c >> 3)) * 18) * 2; }
      f4 d;
      {
        const unsigned* xp = (const unsigned*)(smem + xoff);
        unsigned xs[9];
        constexpr int XA = layer == 0 ? 4 : 9;
#pragma unroll
        for (int n = 0; n < XA; ++n) xs[n] = xp[n];
        sfor<0, 9>([&](auto Nn) {
          constexpr int n = Nn.value;
          const v4u b0 = diag(dup_lo(xs[n]), mk), b1 = diag(dup_hi(xs[n]), mk);
          if constexpr (n + XA < 9) xs[n + XA] = xp[n + XA];
          if constexpr (n == 0) d = mfma_first2(wb[0], b0, wb[1], b1); else mfma_acc2(d, wb[(2 * n) % GW], b0, wb[(2 * n + 1) % GW], b1);
          if constexpr (layer == 0) __builtin_amdgcn_sched_barrier(0);
          frag(std::integral_constant<int, 2 * n + GW>{});
          frag(std::integral_constant<int, 2 * n + 1 + GW>{});
          if constexpr (layer == 0) __builtin_amdgcn_sched_barrier(0);
        });
      }
      const bool hp = (c >> 3) != 0;
      unsigned ra = L_REDA + ((w * 4 + g) * 32 + (c & 7)) * 4, rb = L_REDA + (4 * w + g + 32 * (c & 7) + 16 * (c >> 3)) * 8;
      unsigned rc_ = L_REDB + (w * 512 + 32 * g + (c & 7)) * 4, rd = L_REDB + (2048 + w * 128 + 32 * g + (c & 7)) * 4;
      unsigned o1 = WS_PB + (tid >> 3) * PB_STRIDE + mu * 128 + (tid & 7) * 16, o2 = lane < 8 ? WS_PB + (32 + w) * PB_STRIDE + mu * 128 + lane * 16 : OOB;
      pin(ra); pin(rb); pin(rc_); pin(rd); pin(o1); pin(o2);
      mfma_fence(d);
      if ((g >> 1) == (c >> 3)) {
        float* rp = (float*)(smem + ra);
#pragma unroll
        for (int r = 0; r < 4; ++r) rp[8 * r] = d[r];
      }
      wg_barrier();
      unsigned hd0, hd1;
      {
        const f2 v = *(const f2*)(smem + rb);
        float t[2] = {v[0], v[1]};
#pragma unroll
        for (int i = 0; i < 2; ++i) t[i] += dpp<0xB1>(t[i]);
#pragma unroll
        for (int i = 0; i < 2; ++i) t[i] += dpp<0x4E>(t[i]);
#pragma unroll
        for (int i = 0; i < 2; ++i) t[i] += dpp<0x141>(t[i]);
        const float y = dpp<0x128>(hp ? t[0] : t[1]), own = hp ? t[1] : t[0];
        const float x = rbf(hp ? y : own), up = rbf(hp ? own : y);
        const float z = 0.7978845608028654f * (x + 0.044715f * x * x * x);
        const float th = 1.f - 2.f * __builtin_amdgcn_rcpf(__expf(2.f * z) + 1.f);
        const float hv = rbf(0.5f * x * (1.f + th)) * up;
        const unsigned hd = pk_bf(hv, hv), ho = __builtin_amdgcn_mov_dpp(hd, 0x128, 0xf, 0xf, true);
        hd0 = hp ? ho : hd; hd1 = hp ? hd : ho;
        f4 e[5];
        const v4u b0 = diag(hd0, mk), b1 = diag(hd1, mk);
#pragma unroll
        for (int bl = 0; bl < 4; ++bl) e[bl] = mfma_first2(wb[(18 + 2 * bl) % GW], b0, wb[(19 + 2 * bl) % GW], b1);
        e[4] = mfma_first(wb[26 % GW], diag(hd, mk));
        mfma_fence5(e);
        if (!hp) {
#pragma unroll
          for (int bl = 0; bl < 4; ++bl) {
            float* q = (float*)(smem + rc_) + 128 * bl;
#pragma unroll
            for (int r = 0; r < 4; ++r) q[8 * r] = e[bl][r];
          }
        }
        if ((g >> 1) == (c >> 3)) {
          float* q = (float*)(smem + rd);
#pragma unroll
          for (int r = 0; r < 4; ++r) q[8 * r] = e[4][r];
        }
      }
      wg_barrier();
      {
        const f2* rp = (const f2*)(smem + L_REDB);
        const unsigned tg = TAG(ph + 3);
        f2 m[4], x[8];
#pragma unroll
        for (int i = 0; i < 4; ++i) m[i] = rp[256 * i + tid];
#pragma unroll
        for (int i = 0; i < 8; ++i) x[i] = rp[1024 + 32 * i + 8 * w + (lane & 7)];
        const f2 a = (m[0] + m[1]) + (m[2] + m[3]);
        v4u g0 = {__float_as_uint(a[0]), tg, __float_as_uint(a[1]), tg};
        __builtin_amdgcn_raw_buffer_store_b128(g0, rsW, o1, 0, AUX_SC1);
        const f2 e = ((x[0] + x[1]) + (x[2] + x[3])) + ((x[4] + x[5]) + (x[6] + x[7]));
        v4u g1 = {__float_as_uint(e[0]), tg, __float_as_uint(e[1]), tg};
        __builtin_amdgcn_raw_buffer_store_b128(g1, rsW, o2, 0, AUX_SC1);
      }
      if constexpr (layer == 0) issue_wq();
    } else {
      if (myr >= 0) {
        reduce_cols(std::integral_constant<int, NWG>{}, TAG(ph + 1), std::false_type{}, TAG(ph + 2));
      }
      if constexpr (layer == 0) issue_wq();
      if (myr >= 0) {
        reduce_cols(std::integral_constant<int, NMLP>{}, TAG(ph + 3), std::integral_constant<bool, layer == 1>{}, TAG(ph + 4));
        if (layer == 1 && myr == 0 && tid == 0) __builtin_amdgcn_raw_buffer_store_b32(tagbase + 16u, rsW, WS_EPOCH, 0, AUX_SC1);
      }
    }
    if constexpr (layer == 0) {
      relane();
      pin(mk);
      if (w < 3) gather_x_ln(TAG(ph + 4), 48);
      if (w == 0) put_tb(TAG(ph + 5), now8());
      wait_wq<0>(wqf);
      q_gemv();
      wg_barrier();
      if (w == 3) {
        q_publish(TAG(ph + 5));
        __builtin_amdgcn_s_sleep(6);
        v4u a[3];
        poll3(a, rsC, WS_QG + head * 1024 + lane * 16, WS_TB + (lane & 3) * 16, OOB, TAG(ph + 5), 0ull);
        q_keep(a[0]);
        keep_tb(a[1]);
      }
      wg_barrier();
    }
  };

  load_q();
  sfor<0, D>([&](auto I) { def_a(VL[I.value]); });
  float* alp_ks = (float*)(smem + L_KS + w * (NL * 4096) + D * 4096) + lane;
#pragma unroll 1
  for (int i = 0; i < NB1; ++i) block(std::integral_constant<int, NT1>{}, std::true_type{}, alp_ks, NR + i * NT1, PACE_B1, true, [](auto) {});
  sfor<D, 2 * NL>([&](auto Qi) {
    constexpr int q = Qi.value;
    wait_vm<4 * (D - 1)>();
    if constexpr (q - D >= NL) tile_l(st, std::integral_constant<int, q - D - NL>{});
    gate(PACE_B1);
    if constexpr (q < NL) send_k(q, NTILE - NL + q);
    else issue_v<(q - NL < D) ? 2 : 0>(VL[q - NL], voffV, rsV, toff(NTILE - 2 * NL + q));
    if constexpr (q - D < NR) tile_r(st, std::integral_constant<int, q - D>{});
  });
#pragma unroll
  for (int i = 0; i < WARM1; ++i) warm(i);
  sfor<NL - D, NL>([&](auto I) {
    wait_vm<4 * (NL - 1 - I.value) + 3 * WARM1 + (I.value == NL - 1 ? 6 : 0)>();
    tile_l(st, I);
    if constexpr (I.value == NL - 2) load_wo(wo1);
  });
  chain(I0{}, wo1);
  relane();
  due = lds_get(lbase + L_FLAG + 28) + PACE_OFF2 * 256 + head * HEAD_STAGGER;
  load_q();
  sfor<0, D>([&](auto I) {
    tile_l(st2, I);
    if constexpr (I.value > 0) gate(PACE_A2);
    send_k(I.value, NR + I.value);
  });
  block(std::integral_constant<int, NTA>{}, std::false_type{}, (float*)(smem + L_REDB) + w * (NTA * 64) + lane, NR, PACE_A2, true, [&](auto S) {
    constexpr int nk = NR + NL - D, ink = nk - NTA, s = S.value;
    constexpr int k = s >= NTA ? ink + (s - NTA) : ((s * ink) / NTA != ((s + 1) * ink) / NTA ? (s * ink) / NTA : -1);
    if constexpr (k >= NR) tile_l(st2, std::integral_constant<int, k - NR + D>{});
    else if constexpr (k >= 0) tile_r(st2, std::integral_constant<int, k>{});
  });
  merge_st2();
  alp_ks = (float*)(smem + L_KS + w * (NL * 4096) + D * 4096) + lane;
#pragma unroll 1
  for (int i = 0; i < NB2; ++i) block(std::integral_constant<int, NT2>{}, std::false_type{}, alp_ks, NR + NTA + i * NT2, PACE_B2, i < NB2 - 1, [](auto) {});
  load_wo(wo2);
  chain(I1{}, wo2);
  asm volatile(".Lfd_code_to:");
}

}

extern "C" void kernel_launch(void* const* d_in, const int* in_sizes, int n_in, void* d_out, int out_size, void* d_ws, size_t ws_size,
                              hipStream_t stream) {
  (void)in_sizes; (void)n_in; (void)out_size; (void)ws_size;
  fused_decode_kernel<<<NWG, 256, 0, stream>>>(
      (const unsigned short*)d_in[0], (const unsigned char*)d_in[1], (const unsigned char*)d_in[2], (const unsigned char*)d_in[3],
      (const unsigned char*)d_in[4], (const unsigned char*)d_in[6], (const unsigned char*)d_in[7], (const unsigned char*)d_in[8],
      (unsigned short*)d_out, (unsigned char*)d_ws);
}
```
